```python
import torch

def _softmax(x, axis=-1):
    return torch.softmax(x, dim=axis)

def _gelu(x):
    return torch.nn.functional.gelu(x, approximate='tanh')

def _silu(x):
    return torch.nn.functional.silu(x)

def _sigmoid(x):
    return torch.sigmoid(x)

def _layernorm(x, axis=-1, eps=1e-05):
    m = x.mean(dim=axis, keepdim=True)
    v = x.var(dim=axis, keepdim=True, unbiased=False)
    return (x - m) / torch.sqrt(v + eps)

def _scatter_add(tgt, idx, src, axis=0):
    return tgt.clone().index_add_(axis, idx, src)

def _segment_sum(src, seg, n):
    out = torch.zeros((n,) + src.shape[1:], dtype=src.dtype)
    return out.index_add_(0, seg, src)

def _pad(x, pw):
    flat = tuple((v for pair in reversed(pw) for v in pair))
    return torch.nn.functional.pad(x, flat)
kernel_name = 'standalone__autoregressive_conv_wavenet__speech'

def setup_inputs():

    _w = lambda a: (lambda t: t.to(torch.bfloat16) if t.is_floating_point() else t)(torch.as_tensor(a))
    B, T, D, K = (2, 4000, 256, 31)
    return {'x': _w((lambda *s: torch.randn(*s, dtype=torch.bfloat16) * 0.1)(B, T, D)), 'w': _w((lambda *s: torch.randn(*s, dtype=torch.bfloat16) * 0.1)(K, D, 2 * D)), 'w_o': _w((lambda *s: torch.randn(*s, dtype=torch.bfloat16) * 0.05)(D, D))}

def reference(x, w, w_o):
    B, T, D = x.shape
    K = w.shape[0]
    dil = 2
    pad = (K - 1) * dil
    xp = _pad(x, ((0, 0), (pad, 0), (0, 0)))
    h = torch.einsum('btc,cd->btd', xp[:, pad:pad + T, :], w[0])
    for i in range(1, K):
        off = pad - i * dil
        h = h + torch.einsum('btc,cd->btd', xp[:, off:off + T, :], w[i])
    f = h[..., :D].clone()
    g = h[..., D:].clone()
    z = torch.tanh(f) * _sigmoid(g)
    return z @ w_o
```

```cpp
#include <hip/hip_runtime.h>
#include <hip/hip_bf16.h>
typedef unsigned short u16;
__device__ __forceinline__ float bf2f(u16 v){ return __uint_as_float(((unsigned)v)<<16); }
__device__ __forceinline__ u16 f2bf(float f){ unsigned u=__float_as_uint(f); u += 0x7fffu + ((u>>16)&1u); return (u16)(u>>16); }
constexpr int TT=16;
__global__ __launch_bounds__(256) void naive_k(const u16* x,const u16* w,const u16* wo,u16* out){
  __shared__ float xs[TT+60][256];
  __shared__ float zs[TT][256];
  int b=blockIdx.x/(4000/TT), t0=(blockIdx.x%(4000/TT))*TT, j=threadIdx.x;
  for(int r=0;r<TT+60;r++){ int t=t0-60+r; xs[r][j]= t>=0? bf2f(x[((size_t)b*4000+t)*256+j]):0.f; }
  __syncthreads();
  float f[TT],g[TT];
  #pragma unroll
  for(int r=0;r<TT;r++){f[r]=0;g[r]=0;}
  for(int i=0;i<31;i++){
    for(int c=0;c<256;c++){
      float wf=bf2f(w[((size_t)i*256+c)*512+j]), wg=bf2f(w[((size_t)i*256+c)*512+256+j]);
      #pragma unroll
      for(int r=0;r<TT;r++){ float xv=xs[r+60-2*i][c]; f[r]+=xv*wf; g[r]+=xv*wg; }
    }
  }
  #pragma unroll
  for(int r=0;r<TT;r++){ float z=tanhf(f[r])*(1.f/(1.f+expf(-g[r]))); zs[r][j]=bf2f(f2bf(z)); }
  __syncthreads();
  float o[TT];
  #pragma unroll
  for(int r=0;r<TT;r++)o[r]=0;
  for(int c=0;c<256;c++){ float wv=bf2f(wo[c*256+j]);
    #pragma unroll
    for(int r=0;r<TT;r++) o[r]+=zs[r][c]*wv; }
  #pragma unroll
  for(int r=0;r<TT;r++) out[((size_t)b*4000+t0+r)*256+j]=f2bf(o[r]);
}
extern "C" void kernel_launch(void* const* d_in, const int* in_sizes, int n_in,
                              void* d_out, int out_size, void* d_ws, size_t ws_size,
                              hipStream_t stream) {
  naive_k<<<2*4000/TT,256,0,stream>>>((const u16*)d_in[0],(const u16*)d_in[1],(const u16*)d_in[2],(u16*)d_out);
}
```

```cpp
#include <hip/hip_runtime.h>
#include <stdint.h>

typedef unsigned short u16;
typedef __attribute__((ext_vector_type(8))) __bf16 bf16x8;
typedef __attribute__((ext_vector_type(2))) __bf16 bf16x2;
typedef __attribute__((ext_vector_type(4))) short s16x4;
typedef __attribute__((ext_vector_type(2))) float f32x2;
typedef __attribute__((ext_vector_type(4))) float f32x4;
typedef __attribute__((ext_vector_type(16))) float f32x16;
typedef __attribute__((ext_vector_type(4))) unsigned u32x4;
#define LDSP __attribute__((address_space(3)))
#define DI __device__ __forceinline__

constexpr int T_ = 4000, D_ = 256, KT = 31;
constexpr int NF = 31;
constexpr int NJ = 63;
constexpr int ROWS = 126;
constexpr unsigned X_BYTES = 2u * T_ * D_ * 2u;
constexpr unsigned W_BYTES = KT * D_ * 512u * 2u;
constexpr unsigned XH_BYTES = NF * 2u * 2u * 128u * 512u;
constexpr unsigned WH_BYTES = NF * 16u * 32768u;
constexpr unsigned YH_BYTES = 252u * NF * 2048u;
constexpr unsigned WO_BYTES = 131072u;
constexpr size_t XH_OFF = 0, WH_OFF = XH_OFF + XH_BYTES, YH_OFF = WH_OFF + WH_BYTES, WO_OFF = YH_OFF + YH_BYTES;
constexpr unsigned OOB = 0x7FFFFF00u;

constexpr double kPi = 3.14159265358979323846264338327950288;
constexpr double cx_cos_small(double x) {
  double t = 1.0, s = 1.0;
  for (int i = 1; i <= 14; ++i) { t *= -x * x / ((2 * i - 1) * (2 * i)); s += t; }
  return s;
}
constexpr double cospi62(int m) {
  m %= 124; if (m < 0) m += 124;
  if (m > 62) m = 124 - m;
  if (m == 31) return 0.0;
  return m > 31 ? -cx_cos_small(kPi * (62 - m) / 62.0) : cx_cos_small(kPi * m / 62.0);
}
constexpr u16 cx_bf16(float f) {
  unsigned u = __builtin_bit_cast(unsigned, f);
  u += 0x7fffu + ((u >> 16) & 1u);
  return (u16)(u >> 16);
}
constexpr float cx_f32(u16 h) { return __builtin_bit_cast(float, (unsigned)h << 16); }
struct CosTab { double v[124]; };
constexpr CosTab make_cos() { CosTab c{}; for (int m = 0; m < 124; ++m) c.v[m] = cospi62(m); return c; }
constexpr double basis(const CosTab& c, int f, int n) {
  int m = ((2 * (f >> 1) + 1) * n) % 124;
  return (f & 1) ? c.v[(m + 93) % 124] : c.v[m];
}
struct alignas(16) FTab { u16 v[16 * 512]; };
struct alignas(16) GTab { u16 v[8 * 512]; };
constexpr FTab make_F() {
  FTab t{}; CosTab c = make_cos();
  for (int mt = 0; mt < 4; ++mt) for (int ks = 0; ks < 2; ++ks) for (int l = 0; l < 64; ++l) for (int j = 0; j < 8; ++j) {
    int f = 16 * mt + (l & 15), n = 32 * ks + 8 * (l >> 4) + j;
    double v = (f < 62 && n < 62) ? basis(c, f, n) : 0.0;
    u16 hi = cx_bf16((float)v), lo = cx_bf16((float)(v - (double)cx_f32(hi)));
    t.v[((mt * 2 + ks) * 2 + 0) * 512 + l * 8 + j] = hi;
    t.v[((mt * 2 + ks) * 2 + 1) * 512 + l * 8 + j] = lo;
  }
  return t;
}
constexpr GTab make_G() {
  GTab t{}; CosTab c = make_cos();
  for (int tt = 0; tt < 2; ++tt) for (int ks = 0; ks < 2; ++ks) for (int l = 0; l < 64; ++l) for (int j = 0; j < 8; ++j) {
    int r = 16 * tt + (l & 15), f = 32 * ks + 8 * (l >> 4) + j;
    double v = f < 62 ? basis(c, f, 30 + r) * (2.0 / 62.0) : 0.0;
    u16 hi = cx_bf16((float)v), lo = cx_bf16((float)(v - (double)cx_f32(hi)));
    t.v[((tt * 2 + ks) * 2 + 0) * 512 + l * 8 + j] = hi;
    t.v[((tt * 2 + ks) * 2 + 1) * 512 + l * 8 + j] = lo;
  }
  return t;
}
__device__ const FTab g_F = make_F();
__device__ const GTab g_G = make_G();

DI unsigned pk(float a, float b) { f32x2 t = {a, b}; return __builtin_bit_cast(unsigned, __builtin_convertvector(t, bf16x2)); }
DI bf16x8 as_bf(u32x4 v) { return __builtin_bit_cast(bf16x8, v); }
DI f32x4 mfma16(bf16x8 a, bf16x8 b, f32x4 c) { return __builtin_amdgcn_mfma_f32_16x16x32_bf16(a, b, c, 0, 0, 0); }
DI f32x16 mfma32(bf16x8 a, bf16x8 b, f32x16 c) { return __builtin_amdgcn_mfma_f32_32x32x16_bf16(a, b, c, 0, 0, 0); }
DI bf16x8 tr2(const char* smem, int off0, int off1) {
  s16x4 a = __builtin_amdgcn_ds_read_tr16_b64_v4i16((LDSP s16x4*)(smem + off0));
  s16x4 b = __builtin_amdgcn_ds_read_tr16_b64_v4i16((LDSP s16x4*)(smem + off1));
  return __builtin_bit_cast(bf16x8, __builtin_shufflevector(a, b, 0, 1, 2, 3, 4, 5, 6, 7));
}
#define RSRC(p, n) __builtin_amdgcn_make_buffer_rsrc((void*)(p), 0, (n), 0x00020000)

__global__ __launch_bounds__(512, 2) void k1_fwd(const u16* __restrict__ x, const u16* __restrict__ w,
                                                  const u16* __restrict__ wo, char* __restrict__ ws) {
  __shared__ __attribute__((aligned(16))) char smem[32768 + 32768 + 16384];
  constexpr int SW = 0, SX = 32768, SF = 65536;
  const int tid = threadIdx.x, l = tid & 63;
  const int wv = __builtin_amdgcn_readfirstlane(tid >> 6);
  const int bid = blockIdx.x;
  const int g = l >> 4, q = (l & 15) >> 2, p = l & 3, n16 = l & 15;
  const int s = q | ((g & 1) << 2);
  const int task = (bid & 7) * 32 + (bid >> 3);
  const int G = task >> 3, dt = task & 7;
  const int blk = task;
  const int b = blk / ROWS, row = blk % ROWS, jb = row >> 1, par = row & 1;

  {
    auto rf = RSRC(g_F.v, 16384);
#pragma unroll
    for (int i = 0; i < 2; ++i)
      __builtin_amdgcn_raw_ptr_buffer_load_lds(rf, (LDSP void*)(smem + SF + (wv * 2 + i) * 1024), 16, (wv * 2 + i) * 1024 + l * 16, 0, 0, 0);
  }
  if (wv < 4) {
    auto rw = RSRC(w, W_BYTES);
#pragma unroll
    for (int i = 0; i < 8; ++i) {
      int tap = wv * 8 + i;
      int st = (tap & 3) | (((tap >> 3) & 1) << 2);
      int lo = (16 * l) ^ (st << 5);
      unsigned off = ((unsigned)(tap * 256 + 32 * (lo >> 7) + G) * 512u + dt * 64u) * 2u + (lo & 127);
      __builtin_amdgcn_raw_ptr_buffer_load_lds(rw, (LDSP void*)(smem + SW + tap * 1024), 16, off, 0, 0, 0);
    }
  } else if (blk < 252) {
    auto rx = RSRC(x, X_BYTES);
#pragma unroll
    for (int i = 0; i < 8; ++i) {
      int pr = (wv - 4) * 8 + i;
      int r = 2 * pr + (l >> 5);
      int sr = (r & 3) | (((r >> 3) & 1) << 2);
      int lo = (16 * (l & 31)) ^ (sr << 5);
      int m = 32 * jb - 30 + r;
      unsigned off = (r < 62 && m >= 0 && m < 2000) ? (unsigned)(b * T_ + 2 * m + par) * 512u + lo : OOB;
      __builtin_amdgcn_raw_ptr_buffer_load_lds(rx, (LDSP void*)(smem + SX + pr * 1024), 16, off, 0, 0, 0);
    }
  } else {
    int wid = (blk - 252) * 4 + (wv - 4);
#pragma unroll
    for (int i = 0; i < 8; ++i) {
      int fr = wid * 8 + i, ww = fr >> 4, tile = (fr >> 3) & 1, ks = fr & 7;
      int e = 32 * ww + 8 * (n16 >> 2) + 4 * tile + (n16 & 3);
      u32x4 v;
#pragma unroll
      for (int j = 0; j < 4; ++j) {
        unsigned a = wo[(32 * ks + 8 * g + 2 * j) * 256 + e], c = wo[(32 * ks + 8 * g + 2 * j + 1) * 256 + e];
        v[j] = a | (c << 16);
      }
      *(u32x4*)(ws + WO_OFF + fr * 1024 + l * 16) = v;
    }
  }
  asm volatile("s_waitcnt vmcnt(0)" ::: "memory");
  __syncthreads();

  if (wv < 4) {
    bf16x8 B[8];
#pragma unroll
    for (int j = 0; j < 8; ++j) {
      int a = SW + (8 * g + q) * 1024 + ((((j & 1) * 128 + wv * 32) ^ (s << 5)) + 8 * p) + (j >> 1) * 256;
      B[j] = tr2(smem, a, a + 4096);
    }
    int d = dt * 64 + wv * 16 + n16, fg = d >> 8, zc = d & 255;
    int del = 16 * (zc >> 3) + 8 * fg + (zc & 7);
    unsigned obase = (unsigned)(del >> 5) * 32768u + (G >> 1) * 2048u + ((G & 1) * 32 + (del & 31)) * 16u;
    auto ro = RSRC(ws + WH_OFF, WH_BYTES);
#pragma unroll
    for (int mt = 0; mt < 4; ++mt) {
      bf16x8 Ah = *(const bf16x8*)(smem + SF + ((mt * 2 + 0) * 2 + 0) * 1024 + l * 16);
      bf16x8 Al = *(const bf16x8*)(smem + SF + ((mt * 2 + 0) * 2 + 1) * 1024 + l * 16);
      f32x4 acc[8];
#pragma unroll
      for (int j = 0; j < 8; ++j) { f32x4 z = {0, 0, 0, 0}; acc[j] = mfma16(Al, B[j], z); }
#pragma unroll
      for (int j = 0; j < 8; ++j) acc[j] = mfma16(Ah, B[j], acc[j]);
#pragma unroll
      for (int r = 0; r < 4; ++r) {
        int k = 8 * mt + 2 * g + (r >> 1);
        u32x4 v = {pk(acc[0][r], acc[1][r]), pk(acc[2][r], acc[3][r]), pk(acc[4][r], acc[5][r]), pk(acc[6][r], acc[7][r])};
        unsigned off = obase + (unsigned)k * (16u * 32768u) + (r & 1) * 1024u;
        __builtin_amdgcn_raw_buffer_store_b128(v, ro, off, 0, 0);
      }
    }
  } else if (blk < 252) {
    const int nh = wv & 1, fh = (wv >> 1) & 1;
    bf16x8 B[2][8];
#pragma unroll
    for (int ks = 0; ks < 2; ++ks)
#pragma unroll
      for (int j = 0; j < 8; ++j) {
        int a = SX + (32 * ks + 8 * g + q) * 512 + ((((j & 3) * 64 + nh * 32) ^ (s << 5)) + 8 * p) + (j >> 2) * 256;
        B[ks][j] = tr2(smem, a, a + 2048);
      }
    unsigned obase = (unsigned)b * (2u * 65536u) + row * 512u + (16 * nh + n16) * 16u;
    auto ro = RSRC(ws + XH_OFF, XH_BYTES);
#pragma unroll
    for (int mm = 0; mm < 2; ++mm) {
      int mt = 2 * fh + mm;
      f32x4 acc[8];
#pragma unroll
      for (int j = 0; j < 8; ++j) acc[j] = f32x4{0, 0, 0, 0};
#pragma unroll
      for (int ks = 0; ks < 2; ++ks) {
        bf16x8 Ah = *(const bf16x8*)(smem + SF + ((mt * 2 + ks) * 2 + 0) * 1024 + l * 16);
        bf16x8 Al = *(const bf16x8*)(smem + SF + ((mt * 2 + ks) * 2 + 1) * 1024 + l * 16);
#pragma unroll
        for (int j = 0; j < 8; ++j) acc[j] = mfma16(Al, B[ks][j], acc[j]);
#pragma unroll
        for (int j = 0; j < 8; ++j) acc[j] = mfma16(Ah, B[ks][j], acc[j]);
      }
#pragma unroll
      for (int r = 0; r < 4; ++r) {
        int k = 8 * mt + 2 * g + (r >> 1);
        u32x4 v = {pk(acc[0][r], acc[1][r]), pk(acc[2][r], acc[3][r]), pk(acc[4][r], acc[5][r]), pk(acc[6][r], acc[7][r])};
        unsigned off = obase + (unsigned)k * (4u * 65536u) + (r & 1) * 65536u;
        __builtin_amdgcn_raw_buffer_store_b128(v, ro, off, 0, 0);
      }
    }
  }
}

__global__ __launch_bounds__(256, 1) void k2_gemm(char* __restrict__ ws) {
  extern __shared__ __attribute__((aligned(16))) char smem[];
  const int tid = threadIdx.x, l = tid & 63;
  const int wv = __builtin_amdgcn_readfirstlane(tid >> 6);
  const int bid = blockIdx.x;
  const int idx = bid >> 3, k = (idx >> 3) * 8 + (bid & 7);
  if (k >= NF) return;
  const int b = (idx >> 2) & 1, dq = idx & 3;
  const int n = l & 31, h = l >> 5;

  auto rx = RSRC(ws + XH_OFF, XH_BYTES);
#pragma unroll
  for (int kc = 0; kc < 4; ++kc)
#pragma unroll
    for (int i = 0; i < 8; ++i) {
      int u = wv * 8 + i, ri = u >> 4, r0 = (u & 15) * 8;
      int r = r0 + (l >> 3), pc = (l & 7) ^ ((r >> 1) & 7);
      int rs = r < ROWS ? r : ROWS - 1;
      unsigned off = (unsigned)((k * 2 + b) * 2 + ri) * 65536u + rs * 512u + kc * 128u + pc * 16u;
      __builtin_amdgcn_raw_ptr_buffer_load_lds(rx, (LDSP void*)(smem + (kc * 2 + ri) * 16384 + r0 * 128), 16, off, 0, 0, 0);
    }
  const u32x4* wp = (const u32x4*)(ws + WH_OFF + (size_t)(k * 16 + dq * 4 + wv) * 32768u) + l;
  u32x4 Wf[16][2];
#pragma unroll
  for (int ks = 0; ks < 16; ++ks) { Wf[ks][0] = wp[(ks * 2) * 64]; Wf[ks][1] = wp[(ks * 2 + 1) * 64]; }
  asm volatile("s_waitcnt vmcnt(0)" ::: "memory");
  __syncthreads();

  f32x16 yr[4], yi[4];
#pragma unroll
  for (int mt = 0; mt < 4; ++mt)
#pragma unroll
    for (int i = 0; i < 16; ++i) { yr[mt][i] = 0.f; yi[mt][i] = 0.f; }
  const int sw = (n >> 1) & 7;
#pragma unroll
  for (int ks = 0; ks < 16; ++ks) {
    u32x4 c = Wf[ks][0], d = Wf[ks][1], dn = d ^ 0x80008000u;
    int a0 = (ks >> 2) * 32768 + n * 128 + (((2 * (ks & 3) + h) ^ sw) * 16);
#pragma unroll
    for (int mt = 0; mt < 4; ++mt) {
      bf16x8 xa = *(const bf16x8*)(smem + a0 + mt * 4096);
      bf16x8 xb = *(const bf16x8*)(smem + a0 + mt * 4096 + 16384);
      yr[mt] = mfma32(as_bf(c), xa, yr[mt]);
      yi[mt] = mfma32(as_bf(d), xa, yi[mt]);
      yr[mt] = mfma32(as_bf(dn), xb, yr[mt]);
      yi[mt] = mfma32(as_bf(c), xb, yi[mt]);
    }
  }
  char* st = smem + 131072 + wv * 4096;
  auto ry = RSRC(ws + YH_OFF, YH_BYTES);
#pragma unroll
  for (int mt = 0; mt < 4; ++mt) {
#pragma unroll
    for (int qq = 0; qq < 4; ++qq) {
      u32x4 v = {pk(yr[mt][4 * qq], yi[mt][4 * qq]), pk(yr[mt][4 * qq + 1], yi[mt][4 * qq + 1]),
                 pk(yr[mt][4 * qq + 2], yi[mt][4 * qq + 2]), pk(yr[mt][4 * qq + 3], yi[mt][4 * qq + 3])};
      *(u32x4*)(st + n * 128 + (((2 * qq + h) ^ (n & 7)) * 16)) = v;
    }
#pragma unroll
    for (int i = 0; i < 4; ++i) {
      int r = i * 8 + (l >> 3);
      u32x4 v = *(const u32x4*)(st + r * 128 + (((l & 7) ^ (r & 7)) * 16));
      int rr = mt * 32 + r;
      unsigned off = rr < ROWS ? ((unsigned)(b * ROWS + rr) * NF + k) * 2048u + (dq * 4 + wv) * 128u + (l & 7) * 16u : OOB;
      __builtin_amdgcn_raw_buffer_store_b128(v, ry, off, 0, 0);
    }
  }
}

DI float gate(float f, float g) {
  float u = __builtin_amdgcn_fmed3f(f * 2.0f, -60.0f, 60.0f);
  float E = __builtin_amdgcn_exp2f(u * 1.4426950408889634f);
  float H = __builtin_amdgcn_exp2f(g * -1.4426950408889634f);
  float em1 = __builtin_fabsf(u) < 0.015625f ? u * (1.0f + u * (0.5f + u * 0.16666667f)) : E - 1.0f;
  return em1 * __builtin_amdgcn_rcpf((E + 1.0f) * (1.0f + H));
}
__global__ __launch_bounds__(512, 2) void k3_inv(const char* __restrict__ ws, u16* __restrict__ out) {
  __shared__ __attribute__((aligned(16))) char smem[8192 + 16384];
  constexpr int SG = 0, SZ = 8192;
  const int tid = threadIdx.x, l = tid & 63;
  const int wv = __builtin_amdgcn_readfirstlane(tid >> 6);
  const int blk = blockIdx.x;
  const int b = blk / ROWS, row = blk % ROWS, jb = row >> 1, par = row & 1;
  const int g = l >> 4, n16 = l & 15;

  u32x4 gt = *(const u32x4*)((const char*)g_G.v + wv * 1024 + l * 16);
  auto ry = RSRC(ws + YH_OFF, YH_BYTES);
  u32x4 R[2][4];
#pragma unroll
  for (int ks = 0; ks < 2; ++ks)
#pragma unroll
    for (int jq = 0; jq < 4; ++jq) {
      int k = 16 * ks + 4 * g + jq;
      unsigned off = k < NF ? ((unsigned)blk * NF + k) * 2048u + wv * 256u + n16 * 16u : OOB;
      R[ks][jq] = __builtin_amdgcn_raw_buffer_load_b128(ry, off, 0, 0);
    }
  const u32x4* wop = (const u32x4*)(ws + WO_OFF + wv * 16384) + l;
  u32x4 Wo[2][8];
#pragma unroll
  for (int tile = 0; tile < 2; ++tile)
#pragma unroll
    for (int ks = 0; ks < 8; ++ks) Wo[tile][ks] = wop[(tile * 8 + ks) * 64];

  *(u32x4*)(smem + SG + wv * 1024 + l * 16) = gt;
  asm volatile("s_waitcnt lgkmcnt(0)" ::: "memory");
  __builtin_amdgcn_s_barrier();
  asm volatile("" ::: "memory");

  f32x4 acc[2][4];
#pragma unroll
  for (int tt = 0; tt < 2; ++tt)
#pragma unroll
    for (int dj = 0; dj < 4; ++dj) acc[tt][dj] = f32x4{0, 0, 0, 0};
#pragma unroll
  for (int ks = 0; ks < 2; ++ks)
#pragma unroll
    for (int tt = 0; tt < 2; ++tt) {
      bf16x8 Gh = *(const bf16x8*)(smem + SG + ((tt * 2 + ks) * 2 + 0) * 1024 + l * 16);
      bf16x8 Gl = *(const bf16x8*)(smem + SG + ((tt * 2 + ks) * 2 + 1) * 1024 + l * 16);
#pragma unroll
      for (int dj = 0; dj < 4; ++dj) {
        u32x4 a = {R[ks][0][dj], R[ks][1][dj], R[ks][2][dj], R[ks][3][dj]};
        acc[tt][dj] = mfma16(as_bf(a), Gl, acc[tt][dj]);
        acc[tt][dj] = mfma16(as_bf(a), Gh, acc[tt][dj]);
      }
    }
#pragma unroll
  for (int tt = 0; tt < 2; ++tt) {
    float z[8];
#pragma unroll
    for (int r = 0; r < 2; ++r)
#pragma unroll
      for (int dj = 0; dj < 4; ++dj) z[4 * r + dj] = gate(acc[tt][dj][r], acc[tt][dj][r + 2]);
    u32x4 v = {pk(z[0], z[1]), pk(z[2], z[3]), pk(z[4], z[5]), pk(z[6], z[7])};
    *(u32x4*)(smem + SZ + (tt * 8 + wv) * 1024 + l * 16) = v;
  }
  asm volatile("s_waitcnt lgkmcnt(0)" ::: "memory");
  __builtin_amdgcn_s_barrier();
  asm volatile("" ::: "memory");

  f32x4 o[2][2];
#pragma unroll
  for (int tt = 0; tt < 2; ++tt) { o[tt][0] = f32x4{0, 0, 0, 0}; o[tt][1] = f32x4{0, 0, 0, 0}; }
#pragma unroll
  for (int ks = 0; ks < 8; ++ks)
#pragma unroll
    for (int tt = 0; tt < 2; ++tt) {
      bf16x8 zf = *(const bf16x8*)(smem + SZ + (tt * 8 + ks) * 1024 + l * 16);
      o[tt][0] = mfma16(as_bf(Wo[0][ks]), zf, o[tt][0]);
      o[tt][1] = mfma16(as_bf(Wo[1][ks]), zf, o[tt][1]);
    }
#pragma unroll
  for (int tt = 0; tt < 2; ++tt) {
    int m = 32 * jb + 16 * tt + n16;
    u32x4 v = {pk(o[tt][0][0], o[tt][0][1]), pk(o[tt][0][2], o[tt][0][3]), pk(o[tt][1][0], o[tt][1][1]), pk(o[tt][1][2], o[tt][1][3])};
    if (m < 2000) *(u32x4*)(out + (size_t)(b * T_ + 2 * m + par) * 256 + 32 * wv + 8 * g) = v;
  }
}

extern "C" void kernel_launch(void* const* d_in, const int* in_sizes, int n_in,
                              void* d_out, int out_size, void* d_ws, size_t ws_size,
                              hipStream_t stream) {
  const u16* x = (const u16*)d_in[0];
  const u16* w = (const u16*)d_in[1];
  const u16* wo = (const u16*)d_in[2];
  char* ws = (char*)d_ws;
  constexpr int K2_LDS = 131072 + 16384;
  (void)hipFuncSetAttribute((const void*)k2_gemm, hipFuncAttributeMaxDynamicSharedMemorySize, K2_LDS);
  k1_fwd<<<256, 512, 0, stream>>>(x, w, wo, ws);
  k2_gemm<<<256, 256, K2_LDS, stream>>>(ws);
  k3_inv<<<252, 512, 0, stream>>>(ws, (u16*)d_out);
}
```

```cpp
#define AUX_ST 16
#define AUX_IN 2
#define AUX_Y 2
#define AUX_OUT 2
#define AUX_XH 2
#include <hip/hip_runtime.h>
#include <stdint.h>
#define WS_BASE 0

typedef unsigned short u16;
typedef __attribute__((ext_vector_type(8))) __bf16 bf16x8;
typedef __attribute__((ext_vector_type(2))) __bf16 bf16x2;
typedef __attribute__((ext_vector_type(4))) short s16x4;
typedef __attribute__((ext_vector_type(2))) float f32x2;
typedef __attribute__((ext_vector_type(4))) float f32x4;
typedef __attribute__((ext_vector_type(16))) float f32x16;
typedef __attribute__((ext_vector_type(4))) unsigned u32x4;
#define LDSP __attribute__((address_space(3)))
#define DI __device__ __forceinline__

constexpr int T_ = 4000, D_ = 256, KT = 31;
constexpr int NF = 31;
constexpr int NJ = 63;
constexpr int ROWS = 126;
constexpr unsigned X_BYTES = 2u * T_ * D_ * 2u;
constexpr unsigned W_BYTES = KT * D_ * 512u * 2u;
constexpr unsigned XH_BYTES = NF * 2u * 2u * 128u * 512u;
constexpr unsigned WH_BYTES = NF * 16u * 32768u;
constexpr unsigned YH_BYTES = 252u * NF * 2048u;
constexpr unsigned WO_BYTES = 131072u;
constexpr size_t XH_OFF = 0, WH_OFF = XH_OFF + XH_BYTES, YH_OFF = WH_OFF + WH_BYTES, WO_OFF = YH_OFF + YH_BYTES;
constexpr unsigned OOB = 0x7FFFFF00u;

constexpr double kPi = 3.14159265358979323846264338327950288;
constexpr double cx_cos_small(double x) {
  double t = 1.0, s = 1.0;
  for (int i = 1; i <= 14; ++i) { t *= -x * x / ((2 * i - 1) * (2 * i)); s += t; }
  return s;
}
constexpr double cospi62(int m) {
  m %= 124; if (m < 0) m += 124;
  if (m > 62) m = 124 - m;
  if (m == 31) return 0.0;
  return m > 31 ? -cx_cos_small(kPi * (62 - m) / 62.0) : cx_cos_small(kPi * m / 62.0);
}
constexpr u16 cx_bf16(float f) {
  unsigned u = __builtin_bit_cast(unsigned, f);
  u += 0x7fffu + ((u >> 16) & 1u);
  return (u16)(u >> 16);
}
constexpr float cx_f32(u16 h) { return __builtin_bit_cast(float, (unsigned)h << 16); }
struct CosTab { double v[124]; };
constexpr CosTab make_cos() { CosTab c{}; for (int m = 0; m < 124; ++m) c.v[m] = cospi62(m); return c; }
constexpr double basis(const CosTab& c, int f, int n) {
  int m = ((2 * (f >> 1) + 1) * n) % 124;
  return (f & 1) ? c.v[(m + 93) % 124] : c.v[m];
}
struct alignas(16) FTab { u16 v[16 * 512]; };
struct alignas(16) GTab { u16 v[8 * 512]; };
constexpr FTab make_F() {
  FTab t{}; CosTab c = make_cos();
  for (int mt = 0; mt < 4; ++mt) for (int ks = 0; ks < 2; ++ks) for (int l = 0; l < 64; ++l) for (int j = 0; j < 8; ++j) {
    int f = 16 * mt + (l & 15), n = 32 * ks + 8 * (l >> 4) + j;
    double v = (f < 62 && n < 62) ? basis(c, f, n) : 0.0;
    u16 hi = cx_bf16((float)v), lo = cx_bf16((float)(v - (double)cx_f32(hi)));
    t.v[((mt * 2 + ks) * 2 + 0) * 512 + l * 8 + j] = hi;
    t.v[((mt * 2 + ks) * 2 + 1) * 512 + l * 8 + j] = lo;
  }
  return t;
}
constexpr GTab make_G() {
  GTab t{}; CosTab c = make_cos();
  for (int tt = 0; tt < 2; ++tt) for (int ks = 0; ks < 2; ++ks) for (int l = 0; l < 64; ++l) for (int j = 0; j < 8; ++j) {
    int r = 16 * tt + (l & 15), f = 32 * ks + 8 * (l >> 4) + j;
    double v = f < 62 ? basis(c, f, 30 + r) * (2.0 / 62.0) : 0.0;
    u16 hi = cx_bf16((float)v), lo = cx_bf16((float)(v - (double)cx_f32(hi)));
    t.v[((tt * 2 + ks) * 2 + 0) * 512 + l * 8 + j] = hi;
    t.v[((tt * 2 + ks) * 2 + 1) * 512 + l * 8 + j] = lo;
  }
  return t;
}
__device__ const FTab g_F = make_F();
__device__ const GTab g_G = make_G();

#define STAMP(kid, i)
#define STAMP_DECL
#define STAMP_FLUSH(kid, wsp)
DI unsigned pk(float a, float b) { f32x2 t = {a, b}; return __builtin_bit_cast(unsigned, __builtin_convertvector(t, bf16x2)); }
DI bf16x8 as_bf(u32x4 v) { return __builtin_bit_cast(bf16x8, v); }
DI f32x4 mfma16(bf16x8 a, bf16x8 b, f32x4 c) { return __builtin_amdgcn_mfma_f32_16x16x32_bf16(a, b, c, 0, 0, 0); }
DI f32x16 mfma32(bf16x8 a, bf16x8 b, f32x16 c) { return __builtin_amdgcn_mfma_f32_32x32x16_bf16(a, b, c, 0, 0, 0); }
DI bf16x8 tr2(const char* smem, int off0, int off1) {
  s16x4 a = __builtin_amdgcn_ds_read_tr16_b64_v4i16((LDSP s16x4*)(smem + off0));
  s16x4 b = __builtin_amdgcn_ds_read_tr16_b64_v4i16((LDSP s16x4*)(smem + off1));
  return __builtin_bit_cast(bf16x8, __builtin_shufflevector(a, b, 0, 1, 2, 3, 4, 5, 6, 7));
}
#define RSRC(p, n) __builtin_amdgcn_make_buffer_rsrc((void*)(p), 0, (n), 0x00020000)

__global__ __launch_bounds__(512, 2) void k1_fwd(const u16* __restrict__ x, const u16* __restrict__ w,
                                                  const u16* __restrict__ wo, char* __restrict__ ws) {
  __shared__ __attribute__((aligned(16))) char smem[32768 + 32768 + 16384];
  constexpr int SW = 0, SX = 32768, SF = 65536;
  const int tid = threadIdx.x, l = tid & 63;
  const int wv = __builtin_amdgcn_readfirstlane(tid >> 6);
  const int bid = blockIdx.x;
  const int g = l >> 4, q = (l & 15) >> 2, p = l & 3, n16 = l & 15;
  const int s = q | ((g & 1) << 2);
  const int task = (bid & 7) * 32 + (bid >> 3);
  const int G = task >> 3, dt = task & 7;
  const int blk = task;
  const int b = blk / ROWS, row = blk % ROWS, jb = row >> 1, par = row & 1;

  STAMP_DECL; STAMP(0, 0);
  {
    auto rf = RSRC(g_F.v, 16384);
#pragma unroll
    for (int i = 0; i < 2; ++i)
      __builtin_amdgcn_raw_ptr_buffer_load_lds(rf, (LDSP void*)(smem + SF + (wv * 2 + i) * 1024), 16, (wv * 2 + i) * 1024 + l * 16, 0, 0, 0);
  }
  if (wv < 4) {
    auto rw = RSRC(w, W_BYTES);
#pragma unroll
    for (int i = 0; i < 8; ++i) {
      int tap = wv * 8 + i;
      int st = (tap & 3) | (((tap >> 3) & 1) << 2);
      int lo = (16 * l) ^ (st << 5);
      unsigned off = ((unsigned)(tap * 256 + 32 * (lo >> 7) + G) * 512u + dt * 64u) * 2u + (lo & 127);
      __builtin_amdgcn_raw_ptr_buffer_load_lds(rw, (LDSP void*)(smem + SW + tap * 1024), 16, off, 0, 0, AUX_IN);
    }
  } else if (blk < 252) {
    auto rx = RSRC(x, X_BYTES);
#pragma unroll
    for (int i = 0; i < 8; ++i) {
      int pr = (wv - 4) * 8 + i;
      int r = 2 * pr + (l >> 5);
      int sr = (r & 3) | (((r >> 3) & 1) << 2);
      int lo = (16 * (l & 31)) ^ (sr << 5);
      int m = 32 * jb - 30 + r;
      unsigned off = (r < 62 && m >= 0 && m < 2000) ? (unsigned)(b * T_ + 2 * m + par) * 512u + lo : OOB;
      __builtin_amdgcn_raw_ptr_buffer_load_lds(rx, (LDSP void*)(smem + SX + pr * 1024), 16, off, 0, 0, AUX_IN);
    }
  } else {
    int wid = (blk - 252) * 4 + (wv - 4);
#pragma unroll
    for (int i = 0; i < 8; ++i) {
      int fr = wid * 8 + i, ww = fr >> 4, tile = (fr >> 3) & 1, ks = fr & 7;
      int e = 32 * ww + 8 * (n16 >> 2) + 4 * tile + (n16 & 3);
      u32x4 v;
#pragma unroll
      for (int j = 0; j < 4; ++j) {
        unsigned a = wo[(32 * ks + 8 * g + 2 * j) * 256 + e], c = wo[(32 * ks + 8 * g + 2 * j + 1) * 256 + e];
        v[j] = a | (c << 16);
      }
      *(u32x4*)(ws + WO_OFF + fr * 1024 + l * 16) = v;
    }
  }
  STAMP(0, 1);
  asm volatile("s_waitcnt vmcnt(0)" ::: "memory");
  STAMP(0, 2);
  __syncthreads();
  STAMP(0, 3);

  if (wv < 4) {
    bf16x8 B[8];
#pragma unroll
    for (int j = 0; j < 8; ++j) {
      int a = SW + (8 * g + q) * 1024 + ((((j & 1) * 128 + wv * 32) ^ (s << 5)) + 8 * p) + (j >> 1) * 256;
      B[j] = tr2(smem, a, a + 4096);
    }
    int d = dt * 64 + wv * 16 + n16, fg = d >> 8, zc = d & 255;
    int del = 16 * (zc >> 3) + 8 * fg + (zc & 7);
    unsigned obase = (unsigned)(del >> 5) * 32768u + (G >> 1) * 2048u + ((G & 1) * 32 + (del & 31)) * 16u;
    auto ro = RSRC(ws + WH_OFF, WH_BYTES);
#pragma unroll
    for (int mt = 0; mt < 4; ++mt) {
      bf16x8 Ah = *(const bf16x8*)(smem + SF + ((mt * 2 + 0) * 2 + 0) * 1024 + l * 16);
      bf16x8 Al = *(const bf16x8*)(smem + SF + ((mt * 2 + 0) * 2 + 1) * 1024 + l * 16);
      f32x4 acc[8];
#pragma unroll
      for (int j = 0; j < 8; ++j) { f32x4 z = {0, 0, 0, 0}; acc[j] = mfma16(Al, B[j], z); }
#pragma unroll
      for (int j = 0; j < 8; ++j) acc[j] = mfma16(Ah, B[j], acc[j]);
#pragma unroll
      for (int r = 0; r < 4; ++r) {
        int k = 8 * mt + 2 * g + (r >> 1);
        u32x4 v = {pk(acc[0][r], acc[1][r]), pk(acc[2][r], acc[3][r]), pk(acc[4][r], acc[5][r]), pk(acc[6][r], acc[7][r])};
        unsigned off = obase + (unsigned)k * (16u * 32768u) + (r & 1) * 1024u;
        __builtin_amdgcn_raw_buffer_store_b128(v, ro, off, 0, AUX_ST);
      }
    }
  } else if (blk < 252) {
    const int nh = wv & 1, fh = (wv >> 1) & 1;
    bf16x8 B[2][8];
#pragma unroll
    for (int ks = 0; ks < 2; ++ks)
#pragma unroll
      for (int j = 0; j < 8; ++j) {
        int a = SX + (32 * ks + 8 * g + q) * 512 + ((((j & 3) * 64 + nh * 32) ^ (s << 5)) + 8 * p) + (j >> 2) * 256;
        B[ks][j] = tr2(smem, a, a + 2048);
      }
    unsigned obase = (unsigned)b * (2u * 65536u) + row * 512u + (16 * nh + n16) * 16u;
    auto ro = RSRC(ws + XH_OFF, XH_BYTES);
#pragma unroll
    for (int mm = 0; mm < 2; ++mm) {
      int mt = 2 * fh + mm;
      f32x4 acc[8];
#pragma unroll
      for (int j = 0; j < 8; ++j) acc[j] = f32x4{0, 0, 0, 0};
#pragma unroll
      for (int ks = 0; ks < 2; ++ks) {
        bf16x8 Ah = *(const bf16x8*)(smem + SF + ((mt * 2 + ks) * 2 + 0) * 1024 + l * 16);
        bf16x8 Al = *(const bf16x8*)(smem + SF + ((mt * 2 + ks) * 2 + 1) * 1024 + l * 16);
#pragma unroll
        for (int j = 0; j < 8; ++j) acc[j] = mfma16(Al, B[ks][j], acc[j]);
#pragma unroll
        for (int j = 0; j < 8; ++j) acc[j] = mfma16(Ah, B[ks][j], acc[j]);
      }
#pragma unroll
      for (int r = 0; r < 4; ++r) {
        int k = 8 * mt + 2 * g + (r >> 1);
        u32x4 v = {pk(acc[0][r], acc[1][r]), pk(acc[2][r], acc[3][r]), pk(acc[4][r], acc[5][r]), pk(acc[6][r], acc[7][r])};
        unsigned off = obase + (unsigned)k * (4u * 65536u) + (r & 1) * 65536u;
        __builtin_amdgcn_raw_buffer_store_b128(v, ro, off, 0, AUX_ST);
      }
    }
  }
}

__global__ __launch_bounds__(256, 1) void k2_gemm(char* __restrict__ ws) {
  extern __shared__ __attribute__((aligned(16))) char smem[];
  const int tid = threadIdx.x, l = tid & 63;
  const int wv = __builtin_amdgcn_readfirstlane(tid >> 6);
  const int bid = blockIdx.x;
  const int idx = bid >> 3, k = (idx >> 3) * 8 + (bid & 7);
  if (k >= NF) return;
  const int b = (idx >> 2) & 1, dq = idx & 3;
  const int n = l & 31, h = l >> 5;

  STAMP_DECL; STAMP(1, 0);
  auto rx = RSRC(ws + XH_OFF, XH_BYTES);
#pragma unroll
  for (int kc = 0; kc < 4; ++kc)
#pragma unroll
    for (int i = 0; i < 8; ++i) {
      int u = wv * 8 + i, ri = u >> 4, r0 = (u & 15) * 8;
      int r = r0 + (l >> 3), pc = (l & 7) ^ ((r >> 1) & 7);
      int rs = r < ROWS ? r : ROWS - 1;
      unsigned off = (unsigned)((k * 2 + b) * 2 + ri) * 65536u + rs * 512u + kc * 128u + pc * 16u;
      __builtin_amdgcn_raw_ptr_buffer_load_lds(rx, (LDSP void*)(smem + (kc * 2 + ri) * 16384 + r0 * 128), 16, off, 0, 0, AUX_XH);
    }
  const u32x4* wp = (const u32x4*)(ws + WH_OFF + (size_t)(k * 16 + dq * 4 + wv) * 32768u) + l;
  u32x4 Wf[16][2];
#pragma unroll
  for (int ks = 0; ks < 16; ++ks) { Wf[ks][0] = wp[(ks * 2) * 64]; Wf[ks][1] = wp[(ks * 2 + 1) * 64]; }
  STAMP(1, 1);
  asm volatile("s_waitcnt vmcnt(0)" ::: "memory");
  STAMP(1, 2);
  __syncthreads();
  STAMP(1, 3);

  f32x16 yr[4], yi[4];
#pragma unroll
  for (int mt = 0; mt < 4; ++mt)
#pragma unroll
    for (int i = 0; i < 16; ++i) { yr[mt][i] = 0.f; yi[mt][i] = 0.f; }
  const int sw = (n >> 1) & 7;
#pragma unroll
  for (int ks = 0; ks < 16; ++ks) {
    u32x4 c = Wf[ks][0], d = Wf[ks][1], dn = d ^ 0x80008000u;
    int a0 = (ks >> 2) * 32768 + n * 128 + (((2 * (ks & 3) + h) ^ sw) * 16);
#pragma unroll
    for (int mt = 0; mt < 4; ++mt) {
      bf16x8 xa = *(const bf16x8*)(smem + a0 + mt * 4096);
      bf16x8 xb = *(const bf16x8*)(smem + a0 + mt * 4096 + 16384);
      yr[mt] = mfma32(as_bf(c), xa, yr[mt]);
      yi[mt] = mfma32(as_bf(d), xa, yi[mt]);
      yr[mt] = mfma32(as_bf(dn), xb, yr[mt]);
      yi[mt] = mfma32(as_bf(c), xb, yi[mt]);
    }
  }
  STAMP(1, 4);
  char* st = smem + 131072 + wv * 4096;
  auto ry = RSRC(ws + YH_OFF, YH_BYTES);
#pragma unroll
  for (int mt = 0; mt < 4; ++mt) {
#pragma unroll
    for (int qq = 0; qq < 4; ++qq) {
      u32x4 v = {pk(yr[mt][4 * qq], yi[mt][4 * qq]), pk(yr[mt][4 * qq + 1], yi[mt][4 * qq + 1]),
                 pk(yr[mt][4 * qq + 2], yi[mt][4 * qq + 2]), pk(yr[mt][4 * qq + 3], yi[mt][4 * qq + 3])};
      *(u32x4*)(st + n * 128 + (((2 * qq + h) ^ (n & 7)) * 16)) = v;
    }
#pragma unroll
    for (int i = 0; i < 4; ++i) {
      int r = i * 8 + (l >> 3);
      u32x4 v = *(const u32x4*)(st + r * 128 + (((l & 7) ^ (r & 7)) * 16));
      int rr = mt * 32 + r;
      unsigned off = rr < ROWS ? ((unsigned)(b * ROWS + rr) * NF + k) * 2048u + (dq * 4 + wv) * 128u + (l & 7) * 16u : OOB;
      __builtin_amdgcn_raw_buffer_store_b128(v, ry, off, 0, AUX_ST);
    }
  }
}

DI float gate(float f, float g) {
  float u = __builtin_amdgcn_fmed3f(f * 2.0f, -60.0f, 60.0f);
  float E = __builtin_amdgcn_exp2f(u * 1.4426950408889634f);
  float H = __builtin_amdgcn_exp2f(g * -1.4426950408889634f);
  float em1 = __builtin_fabsf(u) < 0.015625f ? u * (1.0f + u * (0.5f + u * 0.16666667f)) : E - 1.0f;
  return em1 * __builtin_amdgcn_rcpf((E + 1.0f) * (1.0f + H));
}
__global__ __launch_bounds__(512, 2) void k3_inv(const char* __restrict__ ws, u16* __restrict__ out) {
  __shared__ __attribute__((aligned(16))) char smem[8192 + 16384];
  constexpr int SG = 0, SZ = 8192;
  const int tid = threadIdx.x, l = tid & 63;
  const int wv = __builtin_amdgcn_readfirstlane(tid >> 6);
  const int blk = blockIdx.x;
  const int b = blk / ROWS, row = blk % ROWS, jb = row >> 1, par = row & 1;
  const int g = l >> 4, n16 = l & 15;

  STAMP_DECL; STAMP(2, 0);
  u32x4 gt = *(const u32x4*)((const char*)g_G.v + wv * 1024 + l * 16);
  auto ry = RSRC(ws + YH_OFF, YH_BYTES);
  u32x4 R[2][4];
#pragma unroll
  for (int ks = 0; ks < 2; ++ks)
#pragma unroll
    for (int jq = 0; jq < 4; ++jq) {
      int k = 16 * ks + 4 * g + jq;
      unsigned off = k < NF ? ((unsigned)blk * NF + k) * 2048u + wv * 256u + n16 * 16u : OOB;
      R[ks][jq] = __builtin_amdgcn_raw_buffer_load_b128(ry, off, 0, AUX_Y);
    }
  const u32x4* wop = (const u32x4*)(ws + WO_OFF + wv * 16384) + l;
  u32x4 Wo[2][8];
#pragma unroll
  for (int tile = 0; tile < 2; ++tile)
#pragma unroll
    for (int ks = 0; ks < 8; ++ks) Wo[tile][ks] = wop[(tile * 8 + ks) * 64];

  STAMP(2, 1);
  *(u32x4*)(smem + SG + wv * 1024 + l * 16) = gt;
  asm volatile("s_waitcnt lgkmcnt(0)" ::: "memory");
  __builtin_amdgcn_s_barrier();
  asm volatile("" ::: "memory");
  STAMP(2, 2);

  f32x4 acc[2][4];
#pragma unroll
  for (int tt = 0; tt < 2; ++tt)
#pragma unroll
    for (int dj = 0; dj < 4; ++dj) acc[tt][dj] = f32x4{0, 0, 0, 0};
#pragma unroll
  for (int ks = 0; ks < 2; ++ks)
#pragma unroll
    for (int tt = 0; tt < 2; ++tt) {
      bf16x8 Gh = *(const bf16x8*)(smem + SG + ((tt * 2 + ks) * 2 + 0) * 1024 + l * 16);
      bf16x8 Gl = *(const bf16x8*)(smem + SG + ((tt * 2 + ks) * 2 + 1) * 1024 + l * 16);
#pragma unroll
      for (int dj = 0; dj < 4; ++dj) {
        u32x4 a = {R[ks][0][dj], R[ks][1][dj], R[ks][2][dj], R[ks][3][dj]};
        acc[tt][dj] = mfma16(as_bf(a), Gl, acc[tt][dj]);
        acc[tt][dj] = mfma16(as_bf(a), Gh, acc[tt][dj]);
      }
    }
  STAMP(2, 3);
#pragma unroll
  for (int tt = 0; tt < 2; ++tt) {
    float z[8];
#pragma unroll
    for (int r = 0; r < 2; ++r)
#pragma unroll
      for (int dj = 0; dj < 4; ++dj) z[4 * r + dj] = gate(acc[tt][dj][r], acc[tt][dj][r + 2]);
    u32x4 v = {pk(z[0], z[1]), pk(z[2], z[3]), pk(z[4], z[5]), pk(z[6], z[7])};
    *(u32x4*)(smem + SZ + (tt * 8 + wv) * 1024 + l * 16) = v;
  }
  STAMP(2, 4);
  asm volatile("s_waitcnt lgkmcnt(0)" ::: "memory");
  __builtin_amdgcn_s_barrier();
  asm volatile("" ::: "memory");
  STAMP(2, 5);

  f32x4 o[2][2];
#pragma unroll
  for (int tt = 0; tt < 2; ++tt) { o[tt][0] = f32x4{0, 0, 0, 0}; o[tt][1] = f32x4{0, 0, 0, 0}; }
#pragma unroll
  for (int ks = 0; ks < 8; ++ks)
#pragma unroll
    for (int tt = 0; tt < 2; ++tt) {
      bf16x8 zf = *(const bf16x8*)(smem + SZ + (tt * 8 + ks) * 1024 + l * 16);
      o[tt][0] = mfma16(as_bf(Wo[0][ks]), zf, o[tt][0]);
      o[tt][1] = mfma16(as_bf(Wo[1][ks]), zf, o[tt][1]);
    }
#pragma unroll
  for (int tt = 0; tt < 2; ++tt) {
    int m = 32 * jb + 16 * tt + n16;
    u32x4 v = {pk(o[tt][0][0], o[tt][0][1]), pk(o[tt][0][2], o[tt][0][3]), pk(o[tt][1][0], o[tt][1][1]), pk(o[tt][1][2], o[tt][1][3])};
    __builtin_amdgcn_raw_buffer_store_b128(v, RSRC(out, X_BYTES), m < 2000 ? (unsigned)(b * T_ + 2 * m + par) * 512u + 64 * wv + 16 * g : OOB, 0, AUX_OUT);
  }
}

extern "C" void kernel_launch(void* const* d_in, const int* in_sizes, int n_in,
                              void* d_out, int out_size, void* d_ws, size_t ws_size,
                              hipStream_t stream) {
  const u16* x = (const u16*)d_in[0];
  const u16* w = (const u16*)d_in[1];
  const u16* wo = (const u16*)d_in[2];
  char* ws = (char*)d_ws + WS_BASE;
  constexpr int K2_LDS = 131072 + 16384;
  (void)hipFuncSetAttribute((const void*)k2_gemm, hipFuncAttributeMaxDynamicSharedMemorySize, K2_LDS);
  k1_fwd<<<256, 512, 0, stream>>>(x, w, wo, ws);
  k2_gemm<<<256, 256, K2_LDS, stream>>>(ws);
  k3_inv<<<252, 512, 0, stream>>>(ws, (u16*)d_out);
}
```

```cpp
#include <hip/hip_runtime.h>
#include <stdint.h>

typedef unsigned short u16;
#define LDSP __attribute__((address_space(3)))

constexpr double kPi = 3.14159265358979323846264338327950288;
constexpr double cx_cos_small(double x) {
  double t = 1.0, s = 1.0;
  for (int i = 1; i <= 14; ++i) { t *= -x * x / ((2 * i - 1) * (2 * i)); s += t; }
  return s;
}
constexpr double cospi62(int m) {
  m %= 124; if (m < 0) m += 124;
  if (m > 62) m = 124 - m;
  if (m == 31) return 0.0;
  return m > 31 ? -cx_cos_small(kPi * (62 - m) / 62.0) : cx_cos_small(kPi * m / 62.0);
}
constexpr u16 cx_bf16(float f) {
  unsigned u = __builtin_bit_cast(unsigned, f);
  u += 0x7fffu + ((u >> 16) & 1u);
  return (u16)(u >> 16);
}
constexpr float cx_f32(u16 h) { return __builtin_bit_cast(float, (unsigned)h << 16); }
struct CosTab { double v[124]; };
constexpr CosTab make_cos() { CosTab c{}; for (int m = 0; m < 124; ++m) c.v[m] = cospi62(m); return c; }
constexpr double basis(const CosTab& c, int f, int n) {
  int m = ((2 * (f >> 1) + 1) * n) % 124;
  return (f & 1) ? c.v[(m + 93) % 124] : c.v[m];
}
struct alignas(128) Tab { u16 v[40 * 512]; };
constexpr void put(Tab& t, int frag, int l, int j, double v) {
  u16 hi = cx_bf16((float)v), lo = cx_bf16((float)(v - (double)cx_f32(hi)));
  t.v[frag * 512 + l * 8 + j] = hi;
  t.v[(frag + 1) * 512 + l * 8 + j] = lo;
}
constexpr Tab make_tab() {
  Tab t{}; CosTab c = make_cos();
  for (int l = 0; l < 64; ++l) for (int j = 0; j < 8; ++j) {
    for (int mt = 0; mt < 4; ++mt) {
      const int f = 16 * mt + (l & 15);
      for (int fg = 0; fg < 2; ++fg) {
        const int n = 8 * (l >> 4) + j;
        put(t, (fg * 4 + mt) * 2, l, j, (f < 62 && n < 31) ? basis(c, f, n) * (fg ? -1.44269504088896340736 : 2.88539008177792681472) : 0.0);
      }
      for (int ks = 0; ks < 2; ++ks) {
        const int n = 32 * ks + 8 * (l >> 4) + j;
        put(t, 16 + (mt * 2 + ks) * 2, l, j, (f < 62 && n < 62) ? basis(c, f, n) : 0.0);
      }
    }
    for (int ks = 0; ks < 4; ++ks) {
      const int f = 16 * ks + 8 * (l >> 5) + j;
      put(t, 32 + ks * 2, l, j, f < 62 ? basis(c, f, 30 + (l & 31)) * (2.0 / 62.0) : 0.0);
    }
  }
  return t;
}
__device__ __attribute__((used)) const Tab g_T asm("wn_tab") = make_tab();


__global__ __launch_bounds__(512, 2) void k1_fwd(const u16* __restrict__ x, const u16* __restrict__ w,
                                                  const u16* __restrict__ wo, char* __restrict__ ws) {
  __shared__ __attribute__((aligned(16))) char smem[90112];
  asm volatile(
      "s_getpc_b64 s[60:61]\n"
      "wn_code_lo:\n"
      "s_load_dwordx8 s[40:47], %[ka], 0\n"
      "v_min_u32 v4, 43, %[tid]\n"
      "s_bfe_u32 s62, %[bid], 0x20003\n"
      "s_mulk_i32 s62, 44\n"
      "v_add_lshl_u32 v4, s62, v4, 7\n"
      "v_min_u32 v5, wn_code_hi-wn_code_lo-4, v4\n"
      "global_load_dword v198, v5, s[60:61]\n"
      "L_nopf_%=:\n"
      "s_getpc_b64 s[50:51]\n"
      "s_add_u32 s38, s50, wn_tab@rel32@lo+4\n"
      "s_addc_u32 s39, s51, wn_tab@rel32@hi+12\n"
      "v_min_u32 v4, 15, %[tid]\n"
      "s_bfe_u32 s62, %[bid], 0x20003\n"
      "s_mulk_i32 s62, 16\n"
      "v_add_lshl_u32 v4, s62, v4, 7\n"
      "v_min_u32 v5, 0x1f80, v4\n"
      "v_add_u32 v5, 0x8000, v5\n"
      "global_load_dword v199, v5, s[38:39]\n"
      "v_lshrrev_b32 v3, 6, %[tid]\n"
      "v_lshlrev_b32 v1, 4, %[tid]\n"
      "v_readfirstlane_b32 s64, v3\n"
      "v_and_b32 v1, 0x3f0, v1\n"
      "s_and_b32 s66, %[bid], 7\n"
      "s_lshr_b32 s67, %[bid], 3\n"
      "s_cmp_lt_u32 s64, 4\n"
      "s_cbranch_scc0 L_x_%=\n"
      "s_lshr_b32 s68, %[bid], 6\n"
      "s_lshl2_add_u32 s68, s66, s68\n"
      "s_and_b32 s69, s67, 7\n"
      "s_and_b32 s71, s64, 1\n"
      "s_lshl_b32 s71, s71, 7\n"
      "v_xor_b32 v3, s71, v1\n"
      "v_lshrrev_b32 v4, 7, v3\n"
      "v_and_b32 v3, 127, v3\n"
      "v_lshl_or_b32 v8, v4, 15, v3\n"
      "v_xor_b32 v9, 32, v8\n"
      "v_xor_b32 v10, 64, v8\n"
      "v_xor_b32 v11, 96, v8\n"
      "s_lshr_b32 s73, s69, 2\n"
      "s_lshl_b32 s71, s73, 13\n"
      "s_add_u32 s52, s38, s71\n"
      "s_addc_u32 s53, s39, 0\n"
      "s_and_b32 s53, s53, 0xffff\n"
      "s_mov_b32 s54, 8192\n"
      "s_mov_b32 s55, 0x20000\n"
      "s_lshl_b32 s72, s64, 11\n"
      "s_add_u32 s71, s72, %[lds0]\n"
      "s_add_u32 m0, s71, 81920\n"
      "s_lshl_b32 s70, s64, 21\n"
      "buffer_load_dwordx4 v1, s[52:55], s72 offen lds\n"
      "buffer_load_dwordx4 v1, s[52:55], s72 offen offset:1024 lds\n"
      "s_lshl_b32 s71, s68, 10\n"
      "s_add_u32 s70, s70, s71\n"
      "s_lshl_b32 s71, s69, 7\n"
      "s_add_u32 s70, s70, s71\n"
      "s_lshl_b32 s71, s64, 13\n"
      "s_add_u32 s71, s71, %[lds0]\n"
      "s_cmp_eq_u32 s64, 3\n"
      "s_cselect_b32 s72, 0x7fffff00, 0\n"
      "v_or_b32 v12, s72, v11\n"
      "s_waitcnt lgkmcnt(0)\n"
      "s_mov_b32 s48, s42\n"
      "s_and_b32 s49, s43, 0xffff\n"
      "s_mov_b32 s50, 8126464\n"
      "s_mov_b32 s51, 0x20000\n"
      "s_add_u32 m0, s71, 0\n"
      "s_nop 0\n"
      "buffer_load_dwordx4 v8, s[48:51], s70 offen nt lds\n"
      "s_add_u32 m0, s71, 1024\n"
      "s_add_u32 s70, s70, 0x40000\n"
      "buffer_load_dwordx4 v9, s[48:51], s70 offen nt lds\n"
      "s_add_u32 m0, s71, 2048\n"
      "s_add_u32 s70, s70, 0x40000\n"
      "buffer_load_dwordx4 v10, s[48:51], s70 offen nt lds\n"
      "s_add_u32 m0, s71, 3072\n"
      "s_add_u32 s70, s70, 0x40000\n"
      "buffer_load_dwordx4 v11, s[48:51], s70 offen nt lds\n"
      "s_add_u32 m0, s71, 4096\n"
      "s_add_u32 s70, s70, 0x40000\n"
      "buffer_load_dwordx4 v8, s[48:51], s70 offen nt lds\n"
      "s_add_u32 m0, s71, 5120\n"
      "s_add_u32 s70, s70, 0x40000\n"
      "buffer_load_dwordx4 v9, s[48:51], s70 offen nt lds\n"
      "s_add_u32 m0, s71, 6144\n"
      "s_add_u32 s70, s70, 0x40000\n"
      "buffer_load_dwordx4 v10, s[48:51], s70 offen nt lds\n"
      "s_add_u32 m0, s71, 7168\n"
      "s_add_u32 s70, s70, 0x40000\n"
      "buffer_load_dwordx4 v12, s[48:51], s70 offen nt lds\n"
      "v_lshrrev_b32 v2, 4, v1\n"
      "v_lshrrev_b32 v3, 4, v2\n"
      "v_bfe_u32 v4, v2, 2, 2\n"
      "v_and_b32 v5, 1, v3\n"
      "v_lshl_or_b32 v5, v5, 2, v4\n"
      "v_xor_b32 v5, s64, v5\n"
      "v_lshlrev_b32 v5, 5, v5\n"
      "v_lshl_add_u32 v4, v3, 3, v4\n"
      "v_lshl_or_b32 v5, v4, 10, v5\n"
      "v_and_b32 v4, 3, v2\n"
      "v_lshl_or_b32 v5, v4, 3, v5\n"
      "v_xor_b32 v6, 128, v5\n"
      "v_add_u32 v8, %[lds0], v5\n"
      "v_add_u32 v9, %[lds0], v6\n"
      "v_add_u32 v10, %[lds0], v1\n"
      "v_add_u32 v10, 0x14000, v10\n"
      "v_bfe_u32 v4, v2, 3, 1\n"
      "v_and_b32 v5, 0x70, v1\n"
      "v_lshl_or_b32 v5, v4, 11, v5\n"
      "v_lshl_or_b32 v11, v3, 20, v5\n"
      "v_cmp_eq_u32 vcc, 3, v3\n"
      "v_mov_b32 v4, 0x7fffff00\n"
      "s_and_b32 s70, s69, 3\n"
      "s_lshl2_add_u32 s70, s70, s64\n"
      "v_cndmask_b32 v12, v11, v4, vcc\n"
      "s_lshl_b32 s70, s70, 15\n"
      "s_lshr_b32 s71, s68, 2\n"
      "s_lshl_b32 s71, s71, 12\n"
      "s_add_u32 s70, s70, s71\n"
      "s_and_b32 s71, s68, 3\n"
      "s_lshl_b32 s71, s71, 8\n"
      "s_add_u32 s70, s70, s71\n"
      "s_lshl_b32 s71, s73, 7\n"
      "s_add_u32 s70, s70, s71\n"
      "s_add_u32 s70, s70, 0x7c0000\n"
      "s_mov_b32 s56, s46\n"
      "s_and_b32 s57, s47, 0xffff\n"
      "s_mov_b32 s58, 0x26c2000\n"
      "s_mov_b32 s59, 0x20000\n"
      "s_waitcnt vmcnt(0) lgkmcnt(0)\n"
      "s_barrier\n"
      "ds_read_b128 v[68:71], v10 offset:1024\n"
      "ds_read_b64_tr_b16 v[32:33], v8 offset:0\n"
      "ds_read_b64_tr_b16 v[34:35], v8 offset:4096\n"
      "ds_read_b64_tr_b16 v[36:37], v9 offset:0\n"
      "ds_read_b64_tr_b16 v[38:39], v9 offset:4096\n"
      "ds_read_b64_tr_b16 v[40:41], v8 offset:256\n"
      "ds_read_b64_tr_b16 v[42:43], v8 offset:4352\n"
      "ds_read_b64_tr_b16 v[44:45], v9 offset:256\n"
      "ds_read_b64_tr_b16 v[46:47], v9 offset:4352\n"
      "ds_read_b64_tr_b16 v[48:49], v8 offset:512\n"
      "ds_read_b64_tr_b16 v[50:51], v8 offset:4608\n"
      "ds_read_b64_tr_b16 v[52:53], v9 offset:512\n"
      "ds_read_b64_tr_b16 v[54:55], v9 offset:4608\n"
      "ds_read_b64_tr_b16 v[56:57], v8 offset:768\n"
      "ds_read_b64_tr_b16 v[58:59], v8 offset:4864\n"
      "ds_read_b64_tr_b16 v[60:61], v9 offset:768\n"
      "ds_read_b64_tr_b16 v[62:63], v9 offset:4864\n"
      "s_waitcnt lgkmcnt(14)\n"
      "v_mfma_f32_16x16x32_bf16 v[96:99], v[68:71], v[32:35], 0\n"
      "ds_read_b128 v[64:67], v10 offset:0\n"
      "s_waitcnt lgkmcnt(13)\n"
      "v_mfma_f32_16x16x32_bf16 v[100:103], v[68:71], v[36:39], 0\n"
      "ds_read_b128 v[72:75], v10 offset:2048\n"
      "s_waitcnt lgkmcnt(12)\n"
      "v_mfma_f32_16x16x32_bf16 v[104:107], v[68:71], v[40:43], 0\n"
      "ds_read_b128 v[76:79], v10 offset:3072\n"
      "s_waitcnt lgkmcnt(11)\n"
      "v_mfma_f32_16x16x32_bf16 v[108:111], v[68:71], v[44:47], 0\n"
      "ds_read_b128 v[80:83], v10 offset:4096\n"
      "s_waitcnt lgkmcnt(10)\n"
      "v_mfma_f32_16x16x32_bf16 v[112:115], v[68:71], v[48:51], 0\n"
      "ds_read_b128 v[84:87], v10 offset:5120\n"
      "s_waitcnt lgkmcnt(9)\n"
      "v_mfma_f32_16x16x32_bf16 v[116:119], v[68:71], v[52:55], 0\n"
      "ds_read_b128 v[88:91], v10 offset:6144\n"
      "s_waitcnt lgkmcnt(8)\n"
      "v_mfma_f32_16x16x32_bf16 v[120:123], v[68:71], v[56:59], 0\n"
      "ds_read_b128 v[92:95], v10 offset:7168\n"
      "s_waitcnt lgkmcnt(7)\n"
      "v_mfma_f32_16x16x32_bf16 v[124:127], v[68:71], v[60:63], 0\n"
      "s_waitcnt lgkmcnt(0)\n"
      "v_mfma_f32_16x16x32_bf16 v[96:99], v[64:67], v[32:35], v[96:99]\n"
      "v_mfma_f32_16x16x32_bf16 v[100:103], v[64:67], v[36:39], v[100:103]\n"
      "v_mfma_f32_16x16x32_bf16 v[104:107], v[64:67], v[40:43], v[104:107]\n"
      "v_mfma_f32_16x16x32_bf16 v[108:111], v[64:67], v[44:47], v[108:111]\n"
      "v_mfma_f32_16x16x32_bf16 v[112:115], v[64:67], v[48:51], v[112:115]\n"
      "v_mfma_f32_16x16x32_bf16 v[116:119], v[64:67], v[52:55], v[116:119]\n"
      "v_mfma_f32_16x16x32_bf16 v[120:123], v[64:67], v[56:59], v[120:123]\n"
      "v_mfma_f32_16x16x32_bf16 v[124:127], v[64:67], v[60:63], v[124:127]\n"
      "v_mfma_f32_16x16x32_bf16 v[128:131], v[76:79], v[32:35], 0\n"
      "v_mfma_f32_16x16x32_bf16 v[128:131], v[72:75], v[32:35], v[128:131]\n"
      "v_mfma_f32_16x16x32_bf16 v[132:135], v[76:79], v[36:39], 0\n"
      "v_mfma_f32_16x16x32_bf16 v[132:135], v[72:75], v[36:39], v[132:135]\n"
      "v_mfma_f32_16x16x32_bf16 v[136:139], v[76:79], v[40:43], 0\n"
      "v_mfma_f32_16x16x32_bf16 v[136:139], v[72:75], v[40:43], v[136:139]\n"
      "v_mfma_f32_16x16x32_bf16 v[140:143], v[76:79], v[44:47], 0\n"
      "v_mfma_f32_16x16x32_bf16 v[140:143], v[72:75], v[44:47], v[140:143]\n"
      "v_cvt_pk_bf16_f32 v16, v96, v100\n"
      "v_cvt_pk_bf16_f32 v17, v104, v108\n"
      "v_cvt_pk_bf16_f32 v18, v112, v116\n"
      "v_mfma_f32_16x16x32_bf16 v[144:147], v[76:79], v[48:51], 0\n"
      "v_cvt_pk_bf16_f32 v19, v120, v124\n"
      "s_add_u32 s72, s70, 0x0\n"
      "s_barrier\n"
      "v_mfma_f32_16x16x32_bf16 v[144:147], v[72:75], v[48:51], v[144:147]\n"
      "buffer_store_dwordx4 v[16:19], v11, s[56:59], s72 offen offset:0 sc1\n"
      "v_cvt_pk_bf16_f32 v20, v97, v101\n"
      "v_cvt_pk_bf16_f32 v21, v105, v109\n"
      "v_mfma_f32_16x16x32_bf16 v[148:151], v[76:79], v[52:55], 0\n"
      "v_cvt_pk_bf16_f32 v22, v113, v117\n"
      "v_cvt_pk_bf16_f32 v23, v121, v125\n"
      "buffer_store_dwordx4 v[20:23], v11, s[56:59], s72 offen offset:1024 sc1\n"
      "v_mfma_f32_16x16x32_bf16 v[148:151], v[72:75], v[52:55], v[148:151]\n"
      "v_cvt_pk_bf16_f32 v24, v98, v102\n"
      "v_cvt_pk_bf16_f32 v25, v106, v110\n"
      "v_cvt_pk_bf16_f32 v26, v114, v118\n"
      "v_mfma_f32_16x16x32_bf16 v[152:155], v[76:79], v[56:59], 0\n"
      "v_cvt_pk_bf16_f32 v27, v122, v126\n"
      "s_add_u32 s72, s70, 0x80000\n"
      "buffer_store_dwordx4 v[24:27], v11, s[56:59], s72 offen offset:0 sc1\n"
      "v_mfma_f32_16x16x32_bf16 v[152:155], v[72:75], v[56:59], v[152:155]\n"
      "v_cvt_pk_bf16_f32 v28, v99, v103\n"
      "v_cvt_pk_bf16_f32 v29, v107, v111\n"
      "v_cvt_pk_bf16_f32 v30, v115, v119\n"
      "v_mfma_f32_16x16x32_bf16 v[156:159], v[76:79], v[60:63], 0\n"
      "v_cvt_pk_bf16_f32 v31, v123, v127\n"
      "buffer_store_dwordx4 v[28:31], v11, s[56:59], s72 offen offset:1024 sc1\n"
      "v_mfma_f32_16x16x32_bf16 v[156:159], v[72:75], v[60:63], v[156:159]\n"
      "v_mfma_f32_16x16x32_bf16 v[96:99], v[84:87], v[32:35], 0\n"
      "v_mfma_f32_16x16x32_bf16 v[96:99], v[80:83], v[32:35], v[96:99]\n"
      "v_mfma_f32_16x16x32_bf16 v[100:103], v[84:87], v[36:39], 0\n"
      "v_mfma_f32_16x16x32_bf16 v[100:103], v[80:83], v[36:39], v[100:103]\n"
      "v_mfma_f32_16x16x32_bf16 v[104:107], v[84:87], v[40:43], 0\n"
      "v_mfma_f32_16x16x32_bf16 v[104:107], v[80:83], v[40:43], v[104:107]\n"
      "v_mfma_f32_16x16x32_bf16 v[108:111], v[84:87], v[44:47], 0\n"
      "v_mfma_f32_16x16x32_bf16 v[108:111], v[80:83], v[44:47], v[108:111]\n"
      "v_cvt_pk_bf16_f32 v16, v128, v132\n"
      "v_cvt_pk_bf16_f32 v17, v136, v140\n"
      "v_cvt_pk_bf16_f32 v18, v144, v148\n"
      "v_mfma_f32_16x16x32_bf16 v[112:115], v[84:87], v[48:51], 0\n"
      "v_cvt_pk_bf16_f32 v19, v152, v156\n"
      "s_add_u32 s72, s70, 0x400000\n"
      "buffer_store_dwordx4 v[16:19], v11, s[56:59], s72 offen offset:0 sc1\n"
      "v_mfma_f32_16x16x32_bf16 v[112:115], v[80:83], v[48:51], v[112:115]\n"
      "v_cvt_pk_bf16_f32 v20, v129, v133\n"
      "v_cvt_pk_bf16_f32 v21, v137, v141\n"
      "v_cvt_pk_bf16_f32 v22, v145, v149\n"
      "v_mfma_f32_16x16x32_bf16 v[116:119], v[84:87], v[52:55], 0\n"
      "v_cvt_pk_bf16_f32 v23, v153, v157\n"
      "buffer_store_dwordx4 v[20:23], v11, s[56:59], s72 offen offset:1024 sc1\n"
      "v_cvt_pk_bf16_f32 v24, v130, v134\n"
      "v_mfma_f32_16x16x32_bf16 v[116:119], v[80:83], v[52:55], v[116:119]\n"
      "v_cvt_pk_bf16_f32 v25, v138, v142\n"
      "v_cvt_pk_bf16_f32 v26, v146, v150\n"
      "v_cvt_pk_bf16_f32 v27, v154, v158\n"
      "v_mfma_f32_16x16x32_bf16 v[120:123], v[84:87], v[56:59], 0\n"
      "s_add_u32 s72, s70, 0x480000\n"
      "buffer_store_dwordx4 v[24:27], v11, s[56:59], s72 offen offset:0 sc1\n"
      "v_cvt_pk_bf16_f32 v28, v131, v135\n"
      "v_mfma_f32_16x16x32_bf16 v[120:123], v[80:83], v[56:59], v[120:123]\n"
      "v_cvt_pk_bf16_f32 v29, v139, v143\n"
      "v_cvt_pk_bf16_f32 v30, v147, v151\n"
      "v_cvt_pk_bf16_f32 v31, v155, v159\n"
      "v_mfma_f32_16x16x32_bf16 v[124:127], v[84:87], v[60:63], 0\n"
      "buffer_store_dwordx4 v[28:31], v11, s[56:59], s72 offen offset:1024 sc1\n"
      "v_mfma_f32_16x16x32_bf16 v[124:127], v[80:83], v[60:63], v[124:127]\n"
      "v_mfma_f32_16x16x32_bf16 v[128:131], v[92:95], v[32:35], 0\n"
      "v_mfma_f32_16x16x32_bf16 v[128:131], v[88:91], v[32:35], v[128:131]\n"
      "v_mfma_f32_16x16x32_bf16 v[132:135], v[92:95], v[36:39], 0\n"
      "v_mfma_f32_16x16x32_bf16 v[132:135], v[88:91], v[36:39], v[132:135]\n"
      "v_mfma_f32_16x16x32_bf16 v[136:139], v[92:95], v[40:43], 0\n"
      "v_mfma_f32_16x16x32_bf16 v[136:139], v[88:91], v[40:43], v[136:139]\n"
      "v_mfma_f32_16x16x32_bf16 v[140:143], v[92:95], v[44:47], 0\n"
      "v_mfma_f32_16x16x32_bf16 v[140:143], v[88:91], v[44:47], v[140:143]\n"
      "v_cvt_pk_bf16_f32 v16, v96, v100\n"
      "v_cvt_pk_bf16_f32 v17, v104, v108\n"
      "v_cvt_pk_bf16_f32 v18, v112, v116\n"
      "v_mfma_f32_16x16x32_bf16 v[144:147], v[92:95], v[48:51], 0\n"
      "v_cvt_pk_bf16_f32 v19, v120, v124\n"
      "s_add_u32 s72, s70, 0x800000\n"
      "buffer_store_dwordx4 v[16:19], v11, s[56:59], s72 offen offset:0 sc1\n"
      "v_mfma_f32_16x16x32_bf16 v[144:147], v[88:91], v[48:51], v[144:147]\n"
      "v_cvt_pk_bf16_f32 v20, v97, v101\n"
      "v_cvt_pk_bf16_f32 v21, v105, v109\n"
      "v_cvt_pk_bf16_f32 v22, v113, v117\n"
      "v_mfma_f32_16x16x32_bf16 v[148:151], v[92:95], v[52:55], 0\n"
      "v_cvt_pk_bf16_f32 v23, v121, v125\n"
      "buffer_store_dwordx4 v[20:23], v11, s[56:59], s72 offen offset:1024 sc1\n"
      "v_cvt_pk_bf16_f32 v24, v98, v102\n"
      "v_mfma_f32_16x16x32_bf16 v[148:151], v[88:91], v[52:55], v[148:151]\n"
      "v_cvt_pk_bf16_f32 v25, v106, v110\n"
      "v_cvt_pk_bf16_f32 v26, v114, v118\n"
      "v_cvt_pk_bf16_f32 v27, v122, v126\n"
      "v_mfma_f32_16x16x32_bf16 v[152:155], v[92:95], v[56:59], 0\n"
      "s_add_u32 s72, s70, 0x880000\n"
      "buffer_store_dwordx4 v[24:27], v11, s[56:59], s72 offen offset:0 sc1\n"
      "v_cvt_pk_bf16_f32 v28, v99, v103\n"
      "v_mfma_f32_16x16x32_bf16 v[152:155], v[88:91], v[56:59], v[152:155]\n"
      "v_cvt_pk_bf16_f32 v29, v107, v111\n"
      "v_cvt_pk_bf16_f32 v30, v115, v119\n"
      "v_cvt_pk_bf16_f32 v31, v123, v127\n"
      "v_mfma_f32_16x16x32_bf16 v[156:159], v[92:95], v[60:63], 0\n"
      "buffer_store_dwordx4 v[28:31], v11, s[56:59], s72 offen offset:1024 sc1\n"
      "v_mfma_f32_16x16x32_bf16 v[156:159], v[88:91], v[60:63], v[156:159]\n"
      "s_nop 7\n"
      "v_cvt_pk_bf16_f32 v16, v128, v132\n"
      "v_cvt_pk_bf16_f32 v17, v136, v140\n"
      "v_cvt_pk_bf16_f32 v18, v144, v148\n"
      "v_cvt_pk_bf16_f32 v19, v152, v156\n"
      "s_add_u32 s72, s70, 0xc00000\n"
      "buffer_store_dwordx4 v[16:19], v11, s[56:59], s72 offen offset:0 sc1\n"
      "v_cvt_pk_bf16_f32 v20, v129, v133\n"
      "v_cvt_pk_bf16_f32 v21, v137, v141\n"
      "v_cvt_pk_bf16_f32 v22, v145, v149\n"
      "v_cvt_pk_bf16_f32 v23, v153, v157\n"
      "buffer_store_dwordx4 v[20:23], v11, s[56:59], s72 offen offset:1024 sc1\n"
      "v_cvt_pk_bf16_f32 v24, v130, v134\n"
      "v_cvt_pk_bf16_f32 v25, v138, v142\n"
      "v_cvt_pk_bf16_f32 v26, v146, v150\n"
      "v_cvt_pk_bf16_f32 v27, v154, v158\n"
      "s_add_u32 s72, s70, 0xc80000\n"
      "buffer_store_dwordx4 v[24:27], v12, s[56:59], s72 offen offset:0 sc1\n"
      "v_cvt_pk_bf16_f32 v28, v131, v135\n"
      "v_cvt_pk_bf16_f32 v29, v139, v143\n"
      "v_cvt_pk_bf16_f32 v30, v147, v151\n"
      "v_cvt_pk_bf16_f32 v31, v155, v159\n"
      "buffer_store_dwordx4 v[28:31], v12, s[56:59], s72 offen offset:1024 sc1\n"
      "s_branch L_end_%=\n"
      "L_x_%=:\n"
      "s_lshl_b32 s65, s66, 5\n"
      "s_add_u32 s65, s65, s67\n"
      "s_sub_u32 s64, s64, 4\n"
      "s_cmp_ge_u32 s65, 252\n"
      "s_cbranch_scc1 L_wo_%=\n"
      "s_add_u32 s52, s38, 16384\n"
      "s_addc_u32 s53, s39, 0\n"
      "s_and_b32 s53, s53, 0xffff\n"
      "s_mov_b32 s54, 16384\n"
      "s_mov_b32 s55, 0x20000\n"
      "s_lshl_b32 s72, s64, 12\n"
      "s_add_u32 s74, s72, %[lds0]\n"
      "s_add_u32 m0, s74, 65536\n"
      "s_nop 0\n"
      "buffer_load_dwordx4 v1, s[52:55], s72 offen offset:0 lds\n"
      "buffer_load_dwordx4 v1, s[52:55], s72 offen offset:1024 lds\n"
      "buffer_load_dwordx4 v1, s[52:55], s72 offen offset:2048 lds\n"
      "buffer_load_dwordx4 v1, s[52:55], s72 offen offset:3072 lds\n"
      "s_cmp_ge_u32 s65, 126\n"
      "s_cselect_b32 s66, 1, 0\n"
      "s_mul_i32 s67, s66, 126\n"
      "s_sub_u32 s67, s65, s67\n"
      "s_and_b32 s68, s67, 1\n"
      "s_lshr_b32 s69, s67, 1\n"
      "s_lshl_b32 s71, s69, 5\n"
      "s_lshl4_add_u32 s71, s64, s71\n"
      "s_sub_u32 s71, s71, 30\n"
      "s_lshl_b32 s71, s71, 10\n"
      "v_lshrrev_b32 v3, 9, v1\n"
      "v_and_b32 v4, 0x1f0, v1\n"
      "v_lshlrev_b32 v5, 5, v3\n"
      "v_xor_b32 v4, v4, v5\n"
      "v_lshl_or_b32 v4, v3, 10, v4\n"
      "v_xor_b32 v8, 0, v4\n"
      "v_add_u32 v8, s71, v8\n"
      "s_add_u32 s71, s71, 2048\n"
      "v_xor_b32 v9, 64, v4\n"
      "v_add_u32 v9, s71, v9\n"
      "s_add_u32 s71, s71, 2048\n"
      "v_xor_b32 v10, 0, v4\n"
      "v_add_u32 v10, s71, v10\n"
      "s_add_u32 s71, s71, 2048\n"
      "v_xor_b32 v11, 64, v4\n"
      "v_add_u32 v11, s71, v11\n"
      "s_add_u32 s71, s71, 2048\n"
      "v_xor_b32 v12, 128, v4\n"
      "v_add_u32 v12, s71, v12\n"
      "s_add_u32 s71, s71, 2048\n"
      "v_xor_b32 v13, 192, v4\n"
      "v_add_u32 v13, s71, v13\n"
      "s_add_u32 s71, s71, 2048\n"
      "v_xor_b32 v14, 128, v4\n"
      "v_add_u32 v14, s71, v14\n"
      "s_add_u32 s71, s71, 2048\n"
      "v_xor_b32 v15, 192, v4\n"
      "v_add_u32 v15, s71, v15\n"
      "s_mul_i32 s71, s66, 4000\n"
      "s_add_u32 s71, s71, s68\n"
      "s_lshl_b32 s71, s71, 9\n"
      "s_lshl_b32 s73, s68, 9\n"
      "s_sub_u32 s50, 2048000, s73\n"
      "s_mov_b32 s51, 0x20000\n"
      "s_lshl_b32 s73, s64, 13\n"
      "s_add_u32 s73, s73, %[lds0]\n"
      "s_waitcnt lgkmcnt(0)\n"
      "s_add_u32 s48, s40, s71\n"
      "s_addc_u32 s49, s41, 0\n"
      "s_and_b32 s49, s49, 0xffff\n"
      "s_add_u32 m0, s73, 32768\n"
      "s_nop 0\n"
      "buffer_load_dwordx4 v8, s[48:51], 0 offen lds\n"
      "s_add_u32 m0, s73, 33792\n"
      "s_nop 0\n"
      "buffer_load_dwordx4 v9, s[48:51], 0 offen lds\n"
      "s_add_u32 m0, s73, 34816\n"
      "s_nop 0\n"
      "buffer_load_dwordx4 v10, s[48:51], 0 offen lds\n"
      "s_add_u32 m0, s73, 35840\n"
      "s_nop 0\n"
      "buffer_load_dwordx4 v11, s[48:51], 0 offen lds\n"
      "s_add_u32 m0, s73, 36864\n"
      "s_nop 0\n"
      "buffer_load_dwordx4 v12, s[48:51], 0 offen lds\n"
      "s_add_u32 m0, s73, 37888\n"
      "s_nop 0\n"
      "buffer_load_dwordx4 v13, s[48:51], 0 offen lds\n"
      "s_add_u32 m0, s73, 38912\n"
      "s_nop 0\n"
      "buffer_load_dwordx4 v14, s[48:51], 0 offen lds\n"
      "s_add_u32 m0, s73, 39936\n"
      "s_nop 0\n"
      "buffer_load_dwordx4 v15, s[48:51], 0 offen lds\n"
      "s_and_b32 s68, s64, 1\n"
      "s_lshr_b32 s69, s64, 1\n"
      "v_lshrrev_b32 v2, 4, v1\n"
      "v_lshrrev_b32 v3, 4, v2\n"
      "v_bfe_u32 v4, v2, 2, 2\n"
      "v_and_b32 v5, 1, v3\n"
      "v_lshl_or_b32 v5, v5, 2, v4\n"
      "v_xor_b32 v5, s68, v5\n"
      "v_lshlrev_b32 v5, 5, v5\n"
      "v_lshl_add_u32 v4, v3, 3, v4\n"
      "v_lshl_or_b32 v5, v4, 9, v5\n"
      "v_and_b32 v4, 3, v2\n"
      "v_lshl_or_b32 v5, v4, 3, v5\n"
      "v_xor_b32 v8, 0, v5\n"
      "v_add_u32 v8, %[lds0], v8\n"
      "v_xor_b32 v9, 64, v5\n"
      "v_add_u32 v9, %[lds0], v9\n"
      "v_xor_b32 v10, 128, v5\n"
      "v_add_u32 v10, %[lds0], v10\n"
      "v_xor_b32 v11, 192, v5\n"
      "v_add_u32 v11, %[lds0], v11\n"
      "s_lshl_b32 s71, s69, 13\n"
      "s_add_u32 s71, s71, %[lds0]\n"
      "s_add_u32 s71, s71, 65536\n"
      "v_add_u32 v12, s71, v1\n"
      "s_bfe_u32 s71, s67, 0x30001\n"
      "v_and_b32 v4, 7, v2\n"
      "v_xor_b32 v4, s71, v4\n"
      "v_lshlrev_b32 v4, 4, v4\n"
      "v_bfe_u32 v5, v2, 3, 1\n"
      "v_lshl_or_b32 v4, v5, 15, v4\n"
      "v_lshl_or_b32 v13, v3, 19, v4\n"
      "v_cmp_eq_u32 vcc, 3, v3\n"
      "s_cmp_eq_u32 s69, 1\n"
      "s_cselect_b64 vcc, vcc, 0\n"
      "v_mov_b32 v4, 0x7fffff00\n"
      "v_cndmask_b32 v14, v13, v4, vcc\n"
      "s_lshl_b32 s70, s66, 17\n"
      "s_lshl_b32 s71, s67, 7\n"
      "s_add_u32 s70, s70, s71\n"
      "s_lshl_b32 s71, s68, 16\n"
      "s_add_u32 s70, s70, s71\n"
      "s_lshl_b32 s71, s69, 22\n"
      "s_add_u32 s70, s70, s71\n"
      "s_mov_b32 s56, s46\n"
      "s_and_b32 s57, s47, 0xffff\n"
      "s_mov_b32 s58, 0x26c2000\n"
      "s_mov_b32 s59, 0x20000\n"
      "s_barrier\n"
      "s_waitcnt vmcnt(0) lgkmcnt(0)\n"
      "s_barrier\n"
      "ds_read_b128 v[100:103], v12 offset:1024\n"
      "ds_read_b128 v[96:99], v12 offset:0\n"
      "ds_read_b64_tr_b16 v[32:33], v8 offset:32768\n"
      "ds_read_b64_tr_b16 v[34:35], v8 offset:34816\n"
      "ds_read_b64_tr_b16 v[36:37], v9 offset:32768\n"
      "ds_read_b64_tr_b16 v[38:39], v9 offset:34816\n"
      "ds_read_b64_tr_b16 v[40:41], v10 offset:32768\n"
      "ds_read_b64_tr_b16 v[42:43], v10 offset:34816\n"
      "ds_read_b64_tr_b16 v[44:45], v11 offset:32768\n"
      "ds_read_b64_tr_b16 v[46:47], v11 offset:34816\n"
      "ds_read_b64_tr_b16 v[48:49], v8 offset:33024\n"
      "ds_read_b64_tr_b16 v[50:51], v8 offset:35072\n"
      "ds_read_b64_tr_b16 v[52:53], v9 offset:33024\n"
      "ds_read_b64_tr_b16 v[54:55], v9 offset:35072\n"
      "s_waitcnt lgkmcnt(12)\n"
      "s_waitcnt lgkmcnt(10)\n"
      "v_mfma_f32_16x16x32_bf16 v[128:131], v[100:103], v[32:35], 0\n"
      "v_mfma_f32_16x16x32_bf16 v[128:131], v[96:99], v[32:35], v[128:131]\n"
      "ds_read_b64_tr_b16 v[56:57], v10 offset:33024\n"
      "ds_read_b64_tr_b16 v[58:59], v10 offset:35072\n"
      "s_waitcnt lgkmcnt(10)\n"
      "v_mfma_f32_16x16x32_bf16 v[132:135], v[100:103], v[36:39], 0\n"
      "v_mfma_f32_16x16x32_bf16 v[132:135], v[96:99], v[36:39], v[132:135]\n"
      "ds_read_b64_tr_b16 v[60:61], v11 offset:33024\n"
      "ds_read_b64_tr_b16 v[62:63], v11 offset:35072\n"
      "s_waitcnt lgkmcnt(10)\n"
      "v_mfma_f32_16x16x32_bf16 v[136:139], v[100:103], v[40:43], 0\n"
      "v_mfma_f32_16x16x32_bf16 v[136:139], v[96:99], v[40:43], v[136:139]\n"
      "ds_read_b128 v[108:111], v12 offset:3072\n"
      "s_waitcnt lgkmcnt(9)\n"
      "v_mfma_f32_16x16x32_bf16 v[140:143], v[100:103], v[44:47], 0\n"
      "v_mfma_f32_16x16x32_bf16 v[140:143], v[96:99], v[44:47], v[140:143]\n"
      "ds_read_b128 v[104:107], v12 offset:2048\n"
      "s_waitcnt lgkmcnt(8)\n"
      "v_mfma_f32_16x16x32_bf16 v[144:147], v[100:103], v[48:51], 0\n"
      "v_mfma_f32_16x16x32_bf16 v[144:147], v[96:99], v[48:51], v[144:147]\n"
      "ds_read_b64_tr_b16 v[64:65], v8 offset:49152\n"
      "ds_read_b64_tr_b16 v[66:67], v8 offset:51200\n"
      "s_waitcnt lgkmcnt(8)\n"
      "v_mfma_f32_16x16x32_bf16 v[148:151], v[100:103], v[52:55], 0\n"
      "v_mfma_f32_16x16x32_bf16 v[148:151], v[96:99], v[52:55], v[148:151]\n"
      "ds_read_b64_tr_b16 v[68:69], v9 offset:49152\n"
      "ds_read_b64_tr_b16 v[70:71], v9 offset:51200\n"
      "s_waitcnt lgkmcnt(8)\n"
      "v_mfma_f32_16x16x32_bf16 v[152:155], v[100:103], v[56:59], 0\n"
      "v_mfma_f32_16x16x32_bf16 v[152:155], v[96:99], v[56:59], v[152:155]\n"
      "ds_read_b64_tr_b16 v[72:73], v10 offset:49152\n"
      "ds_read_b64_tr_b16 v[74:75], v10 offset:51200\n"
      "s_waitcnt lgkmcnt(8)\n"
      "v_mfma_f32_16x16x32_bf16 v[156:159], v[100:103], v[60:63], 0\n"
      "v_mfma_f32_16x16x32_bf16 v[156:159], v[96:99], v[60:63], v[156:159]\n"
      "ds_read_b64_tr_b16 v[76:77], v11 offset:49152\n"
      "ds_read_b64_tr_b16 v[78:79], v11 offset:51200\n"
      "s_waitcnt lgkmcnt(8)\n"
      "s_waitcnt lgkmcnt(6)\n"
      "v_mfma_f32_16x16x32_bf16 v[128:131], v[108:111], v[64:67], v[128:131]\n"
      "v_mfma_f32_16x16x32_bf16 v[128:131], v[104:107], v[64:67], v[128:131]\n"
      "ds_read_b64_tr_b16 v[80:81], v8 offset:49408\n"
      "ds_read_b64_tr_b16 v[82:83], v8 offset:51456\n"
      "s_waitcnt lgkmcnt(6)\n"
      "v_mfma_f32_16x16x32_bf16 v[132:135], v[108:111], v[68:71], v[132:135]\n"
      "v_mfma_f32_16x16x32_bf16 v[132:135], v[104:107], v[68:71], v[132:135]\n"
      "ds_read_b64_tr_b16 v[84:85], v9 offset:49408\n"
      "ds_read_b64_tr_b16 v[86:87], v9 offset:51456\n"
      "s_waitcnt lgkmcnt(6)\n"
      "v_mfma_f32_16x16x32_bf16 v[136:139], v[108:111], v[72:75], v[136:139]\n"
      "v_mfma_f32_16x16x32_bf16 v[136:139], v[104:107], v[72:75], v[136:139]\n"
      "ds_read_b64_tr_b16 v[88:89], v10 offset:49408\n"
      "ds_read_b64_tr_b16 v[90:91], v10 offset:51456\n"
      "s_waitcnt lgkmcnt(6)\n"
      "v_mfma_f32_16x16x32_bf16 v[140:143], v[108:111], v[76:79], v[140:143]\n"
      "v_mfma_f32_16x16x32_bf16 v[140:143], v[104:107], v[76:79], v[140:143]\n"
      "ds_read_b64_tr_b16 v[92:93], v11 offset:49408\n"
      "ds_read_b64_tr_b16 v[94:95], v11 offset:51456\n"
      "s_waitcnt lgkmcnt(6)\n"
      "v_mfma_f32_16x16x32_bf16 v[144:147], v[108:111], v[80:83], v[144:147]\n"
      "v_mfma_f32_16x16x32_bf16 v[144:147], v[104:107], v[80:83], v[144:147]\n"
      "ds_read_b128 v[116:119], v12 offset:5120\n"
      "s_waitcnt lgkmcnt(5)\n"
      "v_mfma_f32_16x16x32_bf16 v[148:151], v[108:111], v[84:87], v[148:151]\n"
      "v_mfma_f32_16x16x32_bf16 v[148:151], v[104:107], v[84:87], v[148:151]\n"
      "ds_read_b128 v[112:115], v12 offset:4096\n"
      "s_waitcnt lgkmcnt(4)\n"
      "v_mfma_f32_16x16x32_bf16 v[152:155], v[108:111], v[88:91], v[152:155]\n"
      "v_mfma_f32_16x16x32_bf16 v[152:155], v[104:107], v[88:91], v[152:155]\n"
      "ds_read_b128 v[124:127], v12 offset:7168\n"
      "s_waitcnt lgkmcnt(3)\n"
      "v_mfma_f32_16x16x32_bf16 v[156:159], v[108:111], v[92:95], v[156:159]\n"
      "v_mfma_f32_16x16x32_bf16 v[156:159], v[104:107], v[92:95], v[156:159]\n"
      "ds_read_b128 v[120:123], v12 offset:6144\n"
      "s_waitcnt lgkmcnt(0)\n"
      "v_mfma_f32_16x16x32_bf16 v[160:163], v[116:119], v[32:35], 0\n"
      "v_mfma_f32_16x16x32_bf16 v[160:163], v[112:115], v[32:35], v[160:163]\n"
      "v_mfma_f32_16x16x32_bf16 v[160:163], v[124:127], v[64:67], v[160:163]\n"
      "v_mfma_f32_16x16x32_bf16 v[160:163], v[120:123], v[64:67], v[160:163]\n"
      "v_mfma_f32_16x16x32_bf16 v[164:167], v[116:119], v[36:39], 0\n"
      "v_mfma_f32_16x16x32_bf16 v[164:167], v[112:115], v[36:39], v[164:167]\n"
      "v_mfma_f32_16x16x32_bf16 v[164:167], v[124:127], v[68:71], v[164:167]\n"
      "v_mfma_f32_16x16x32_bf16 v[164:167], v[120:123], v[68:71], v[164:167]\n"
      "v_cvt_pk_bf16_f32 v16, v128, v132\n"
      "v_mfma_f32_16x16x32_bf16 v[168:171], v[116:119], v[40:43], 0\n"
      "v_cvt_pk_bf16_f32 v17, v136, v140\n"
      "v_mfma_f32_16x16x32_bf16 v[168:171], v[112:115], v[40:43], v[168:171]\n"
      "v_cvt_pk_bf16_f32 v18, v144, v148\n"
      "v_mfma_f32_16x16x32_bf16 v[168:171], v[124:127], v[72:75], v[168:171]\n"
      "v_cvt_pk_bf16_f32 v19, v152, v156\n"
      "v_mfma_f32_16x16x32_bf16 v[168:171], v[120:123], v[72:75], v[168:171]\n"
      "s_add_u32 s72, s70, 0x0\n"
      "v_mfma_f32_16x16x32_bf16 v[172:175], v[116:119], v[44:47], 0\n"
      "buffer_store_dwordx4 v[16:19], v13, s[56:59], s72 offen sc1\n"
      "v_mfma_f32_16x16x32_bf16 v[172:175], v[112:115], v[44:47], v[172:175]\n"
      "v_cvt_pk_bf16_f32 v20, v129, v133\n"
      "v_mfma_f32_16x16x32_bf16 v[172:175], v[124:127], v[76:79], v[172:175]\n"
      "v_cvt_pk_bf16_f32 v21, v137, v141\n"
      "v_mfma_f32_16x16x32_bf16 v[172:175], v[120:123], v[76:79], v[172:175]\n"
      "v_cvt_pk_bf16_f32 v22, v145, v149\n"
      "v_mfma_f32_16x16x32_bf16 v[176:179], v[116:119], v[48:51], 0\n"
      "v_cvt_pk_bf16_f32 v23, v153, v157\n"
      "v_mfma_f32_16x16x32_bf16 v[176:179], v[112:115], v[48:51], v[176:179]\n"
      "s_add_u32 s72, s70, 0x4000\n"
      "v_mfma_f32_16x16x32_bf16 v[176:179], v[124:127], v[80:83], v[176:179]\n"
      "buffer_store_dwordx4 v[20:23], v13, s[56:59], s72 offen sc1\n"
      "v_mfma_f32_16x16x32_bf16 v[176:179], v[120:123], v[80:83], v[176:179]\n"
      "v_cvt_pk_bf16_f32 v24, v130, v134\n"
      "v_mfma_f32_16x16x32_bf16 v[180:183], v[116:119], v[52:55], 0\n"
      "v_cvt_pk_bf16_f32 v25, v138, v142\n"
      "v_mfma_f32_16x16x32_bf16 v[180:183], v[112:115], v[52:55], v[180:183]\n"
      "v_cvt_pk_bf16_f32 v26, v146, v150\n"
      "v_mfma_f32_16x16x32_bf16 v[180:183], v[124:127], v[84:87], v[180:183]\n"
      "v_cvt_pk_bf16_f32 v27, v154, v158\n"
      "v_mfma_f32_16x16x32_bf16 v[180:183], v[120:123], v[84:87], v[180:183]\n"
      "s_add_u32 s72, s70, 0x40000\n"
      "v_mfma_f32_16x16x32_bf16 v[184:187], v[116:119], v[56:59], 0\n"
      "buffer_store_dwordx4 v[24:27], v13, s[56:59], s72 offen sc1\n"
      "v_mfma_f32_16x16x32_bf16 v[184:187], v[112:115], v[56:59], v[184:187]\n"
      "v_cvt_pk_bf16_f32 v28, v131, v135\n"
      "v_mfma_f32_16x16x32_bf16 v[184:187], v[124:127], v[88:91], v[184:187]\n"
      "v_cvt_pk_bf16_f32 v29, v139, v143\n"
      "v_mfma_f32_16x16x32_bf16 v[184:187], v[120:123], v[88:91], v[184:187]\n"
      "v_cvt_pk_bf16_f32 v30, v147, v151\n"
      "v_mfma_f32_16x16x32_bf16 v[188:191], v[116:119], v[60:63], 0\n"
      "v_cvt_pk_bf16_f32 v31, v155, v159\n"
      "v_mfma_f32_16x16x32_bf16 v[188:191], v[112:115], v[60:63], v[188:191]\n"
      "s_add_u32 s72, s70, 0x44000\n"
      "v_mfma_f32_16x16x32_bf16 v[188:191], v[124:127], v[92:95], v[188:191]\n"
      "buffer_store_dwordx4 v[28:31], v13, s[56:59], s72 offen sc1\n"
      "v_mfma_f32_16x16x32_bf16 v[188:191], v[120:123], v[92:95], v[188:191]\n"
      "s_nop 7\n"
      "v_cvt_pk_bf16_f32 v16, v160, v164\n"
      "v_cvt_pk_bf16_f32 v17, v168, v172\n"
      "v_cvt_pk_bf16_f32 v18, v176, v180\n"
      "v_cvt_pk_bf16_f32 v19, v184, v188\n"
      "s_add_u32 s72, s70, 0x200000\n"
      "buffer_store_dwordx4 v[16:19], v13, s[56:59], s72 offen sc1\n"
      "v_cvt_pk_bf16_f32 v20, v161, v165\n"
      "v_cvt_pk_bf16_f32 v21, v169, v173\n"
      "v_cvt_pk_bf16_f32 v22, v177, v181\n"
      "v_cvt_pk_bf16_f32 v23, v185, v189\n"
      "s_add_u32 s72, s70, 0x204000\n"
      "buffer_store_dwordx4 v[20:23], v13, s[56:59], s72 offen sc1\n"
      "v_cvt_pk_bf16_f32 v24, v162, v166\n"
      "v_cvt_pk_bf16_f32 v25, v170, v174\n"
      "v_cvt_pk_bf16_f32 v26, v178, v182\n"
      "v_cvt_pk_bf16_f32 v27, v186, v190\n"
      "s_add_u32 s72, s70, 0x240000\n"
      "buffer_store_dwordx4 v[24:27], v14, s[56:59], s72 offen sc1\n"
      "v_cvt_pk_bf16_f32 v28, v163, v167\n"
      "v_cvt_pk_bf16_f32 v29, v171, v175\n"
      "v_cvt_pk_bf16_f32 v30, v179, v183\n"
      "v_cvt_pk_bf16_f32 v31, v187, v191\n"
      "s_add_u32 s72, s70, 0x244000\n"
      "buffer_store_dwordx4 v[28:31], v14, s[56:59], s72 offen sc1\n"
      "s_branch L_end_%=\n"
      "L_wo_%=:\n"
      "s_sub_u32 s66, s65, 252\n"
      "s_lshl2_add_u32 s67, s66, s64\n"
      "s_lshl_b32 s70, s66, 15\n"
      "s_lshl_b32 s71, s64, 7\n"
      "s_add_u32 s70, s70, s71\n"
      "s_lshl_b32 s73, s64, 13\n"
      "s_add_u32 s73, s73, %[lds0]\n"
      "v_lshrrev_b32 v3, 7, v1\n"
      "v_and_b32 v4, 0x70, v1\n"
      "v_lshl_or_b32 v5, v3, 9, v4\n"
      "s_waitcnt lgkmcnt(0)\n"
      "s_mov_b32 s48, s44\n"
      "s_and_b32 s49, s45, 0xffff\n"
      "s_mov_b32 s50, 131072\n"
      "s_mov_b32 s51, 0x20000\n"
      "s_mov_b32 s56, s46\n"
      "s_and_b32 s57, s47, 0xffff\n"
      "s_mov_b32 s58, 0x26c2000\n"
      "s_mov_b32 s59, 0x20000\n"
      "s_add_u32 m0, s73, 32768\n"
      "s_add_u32 s72, s70, 0\n"
      "buffer_load_dwordx4 v5, s[48:51], s72 offen lds\n"
      "s_add_u32 m0, s73, 33792\n"
      "s_add_u32 s72, s70, 4096\n"
      "buffer_load_dwordx4 v5, s[48:51], s72 offen lds\n"
      "s_add_u32 m0, s73, 34816\n"
      "s_add_u32 s72, s70, 8192\n"
      "buffer_load_dwordx4 v5, s[48:51], s72 offen lds\n"
      "s_add_u32 m0, s73, 35840\n"
      "s_add_u32 s72, s70, 12288\n"
      "buffer_load_dwordx4 v5, s[48:51], s72 offen lds\n"
      "s_add_u32 m0, s73, 36864\n"
      "s_add_u32 s72, s70, 16384\n"
      "buffer_load_dwordx4 v5, s[48:51], s72 offen lds\n"
      "s_add_u32 m0, s73, 37888\n"
      "s_add_u32 s72, s70, 20480\n"
      "buffer_load_dwordx4 v5, s[48:51], s72 offen lds\n"
      "s_add_u32 m0, s73, 38912\n"
      "s_add_u32 s72, s70, 24576\n"
      "buffer_load_dwordx4 v5, s[48:51], s72 offen lds\n"
      "s_add_u32 m0, s73, 39936\n"
      "s_add_u32 s72, s70, 28672\n"
      "buffer_load_dwordx4 v5, s[48:51], s72 offen lds\n"
      "v_lshlrev_b32 v3, 3, v1\n"
      "v_mov_b32 v4, 0\n"
      "s_lshl_b32 s72, s67, 13\n"
      "s_add_u32 s72, s72, 0x26a2000\n"
      "buffer_store_dword v4, v3, s[56:59], s72 offen sc1\n"
      "s_barrier\n"
      "v_lshrrev_b32 v2, 4, v1\n"
      "v_lshrrev_b32 v3, 5, v2\n"
      "v_bfe_u32 v4, v2, 2, 2\n"
      "v_lshl_add_u32 v3, v3, 3, v4\n"
      "v_lshlrev_b32 v3, 7, v3\n"
      "v_bfe_u32 v4, v2, 4, 1\n"
      "v_lshl_or_b32 v3, v4, 5, v3\n"
      "v_and_b32 v4, 1, v2\n"
      "v_lshl_or_b32 v3, v4, 4, v3\n"
      "v_bfe_u32 v4, v2, 1, 1\n"
      "v_lshl_or_b32 v3, v4, 3, v3\n"
      "v_add_u32 v8, s73, v3\n"
      "s_lshl_b32 s70, s64, 15\n"
      "s_sub_u32 s71, s66, s64\n"
      "s_and_b32 s71, s71, 3\n"
      "s_lshl_b32 s71, s71, 12\n"
      "s_add_u32 s70, s70, s71\n"
      "s_add_u32 s70, s70, 0x2682000\n"
      "s_add_u32 s72, s70, 16384\n"
      "s_barrier\n"
      "s_waitcnt vmcnt(0)\n"
      "ds_read_b64_tr_b16 v[32:33], v8 offset:32768\n"
      "ds_read_b64_tr_b16 v[34:35], v8 offset:33280\n"
      "ds_read_b64_tr_b16 v[36:37], v8 offset:34816\n"
      "ds_read_b64_tr_b16 v[38:39], v8 offset:35328\n"
      "ds_read_b64_tr_b16 v[40:41], v8 offset:36864\n"
      "ds_read_b64_tr_b16 v[42:43], v8 offset:37376\n"
      "ds_read_b64_tr_b16 v[44:45], v8 offset:38912\n"
      "ds_read_b64_tr_b16 v[46:47], v8 offset:39424\n"
      "ds_read_b64_tr_b16 v[48:49], v8 offset:32832\n"
      "ds_read_b64_tr_b16 v[50:51], v8 offset:33344\n"
      "ds_read_b64_tr_b16 v[52:53], v8 offset:34880\n"
      "ds_read_b64_tr_b16 v[54:55], v8 offset:35392\n"
      "ds_read_b64_tr_b16 v[56:57], v8 offset:36928\n"
      "ds_read_b64_tr_b16 v[58:59], v8 offset:37440\n"
      "ds_read_b64_tr_b16 v[60:61], v8 offset:38976\n"
      "ds_read_b64_tr_b16 v[62:63], v8 offset:39488\n"
      "s_waitcnt lgkmcnt(0)\n"
      "buffer_store_dwordx4 v[32:35], v1, s[56:59], s70 offen offset:0 sc1\n"
      "buffer_store_dwordx4 v[36:39], v1, s[56:59], s70 offen offset:1024 sc1\n"
      "buffer_store_dwordx4 v[40:43], v1, s[56:59], s70 offen offset:2048 sc1\n"
      "buffer_store_dwordx4 v[44:47], v1, s[56:59], s70 offen offset:3072 sc1\n"
      "buffer_store_dwordx4 v[48:51], v1, s[56:59], s72 offen offset:0 sc1\n"
      "buffer_store_dwordx4 v[52:55], v1, s[56:59], s72 offen offset:1024 sc1\n"
      "buffer_store_dwordx4 v[56:59], v1, s[56:59], s72 offen offset:2048 sc1\n"
      "buffer_store_dwordx4 v[60:63], v1, s[56:59], s72 offen offset:3072 sc1\n"
      "L_end_%=:\n"
      :
      : [ka] "s"(__builtin_amdgcn_kernarg_segment_ptr()), [bid] "s"(blockIdx.x), [tid] "v"(threadIdx.x),
        [lds0] "s"((unsigned)(size_t)(LDSP char*)smem)
      : "memory", "scc", "v1", "v2", "v3", "v4", "v5", "v6", "v7", "v8", "v9", "v10", "v11", "v12", "v13", "v14", "v15", "v16",
        "v17", "v18", "v19", "v20", "v21", "v22", "v23", "v24", "v25", "v26", "v27", "v28", "v29", "v30", "v31", "v32",
        "v33", "v34", "v35", "v36", "v37", "v38", "v39", "v40", "v41", "v42", "v43", "v44", "v45", "v46", "v47", "v48",
        "v49", "v50", "v51", "v52", "v53", "v54", "v55", "v56", "v57", "v58", "v59", "v60", "v61", "v62", "v63", "v64",
        "v65", "v66", "v67", "v68", "v69", "v70", "v71", "v72", "v73", "v74", "v75", "v76", "v77", "v78", "v79", "v80",
        "v81", "v82", "v83", "v84", "v85", "v86", "v87", "v88", "v89", "v90", "v91", "v92", "v93", "v94", "v95", "v96",
        "v97", "v98", "v99", "v100", "v101", "v102", "v103", "v104", "v105", "v106", "v107", "v108", "v109", "v110", "v111", "v112",
        "v113", "v114", "v115", "v116", "v117", "v118", "v119", "v120", "v121", "v122", "v123", "v124", "v125", "v126", "v127", "v128",
        "v129", "v130", "v131", "v132", "v133", "v134", "v135", "v136", "v137", "v138", "v139", "v140", "v141", "v142", "v143", "v144",
        "v145", "v146", "v147", "v148", "v149", "v150", "v151", "v152", "v153", "v154", "v155", "v156", "v157", "v158", "v159", "v160",
        "v161", "v162", "v163", "v164", "v165", "v166", "v167", "v168", "v169", "v170", "v171", "v172", "v173", "v174", "v175", "v176",
        "v177", "v178", "v179", "v180", "v181", "v182", "v183", "v184", "v185", "v186", "v187", "v188", "v189", "v190", "v191", "v192",
        "v193", "v194", "v195", "v196", "v197", "v198", "v199", "s34", "s35", "s36", "s37", "s38", "s39", "s40", "s41", "s42",
        "s43", "s44", "s45", "s46", "s47", "s48", "s49", "s50", "s51", "s52", "s53", "s54", "s55", "s56", "s57", "s58",
        "s59", "s60", "s61", "s62", "s63", "s64", "s65", "s66", "s67", "s68", "s69", "s70", "s71", "s72", "s73", "s74",
        "s75", "s76", "s77", "s78", "s79", "s80", "s81", "s82", "s83", "s84", "s85", "s86", "s87", "s88", "s89", "s90",
        "s91", "s92", "s93", "s94", "s95", "s96", "s97", "s98", "s99", "vcc");
}

__global__ __launch_bounds__(256, 1) void k23(char* __restrict__ ws, u16* __restrict__ out) {
  __shared__ __attribute__((aligned(16))) char smem[155648];
  asm volatile(
      "s_load_dwordx4 s[36:39], %[ka], 0\n"
      "v_lshrrev_b32 v23, 6, %[tid]\n"
      "v_lshlrev_b32 v2, 4, %[tid]\n"
      "s_nop 0\n"
      "v_readfirstlane_b32 s52, v23\n"
      "s_mov_b32 s42, 0x26c2000\n"
      "s_mov_b32 s43, 0x20000\n"
      "v_and_b32 v2, 0x3f0, v2\n"
      "s_lshr_b32 s67, %[bid], 6\n"
      "s_lshl_b32 s67, s67, 3\n"
      "s_and_b32 s53, %[bid], 7\n"
      "s_or_b32 s53, s53, s67\n"
      "s_bfe_u32 s54, %[bid], 0x10005\n"
      "s_bfe_u32 s55, %[bid], 0x20003\n"
      "s_lshl_b32 s57, s53, 18\n"
      "s_lshl_b32 s67, s54, 17\n"
      "s_add_u32 s57, s57, s67\n"
      "s_lshl_b32 s67, s52, 12\n"
      "s_add_u32 s56, s57, s67\n"
      "s_add_u32 s58, %[lds0], s67\n"
      "s_lshl_b32 s55, s55, 2\n"
      "s_add_u32 s55, s55, s52\n"
      "s_lshl_b32 s68, s53, 4\n"
      "s_add_u32 s68, s68, s55\n"
      "s_lshl_b32 s68, s68, 15\n"
      "s_add_u32 s60, s68, 0x7c0000\n"
      "v_mov_b32 v23, 0x7fffff00\n"
      "v_cmp_lt_u32 vcc, 0x2ff, v2\n"
      "s_cmp_eq_u32 s52, 3\n"
      "s_cselect_b64 s[98:99], vcc, 0\n"
      "v_cndmask_b32 v4, v2, v23, vcc\n"
      "v_cndmask_b32 v3, v2, v23, s[98:99]\n"
      "s_waitcnt lgkmcnt(0)\n"
      "s_mov_b32 s40, s36\n"
      "s_and_b32 s41, s37, 0xffff\n"
      "s_cmp_eq_u32 s53, 31\n"
      "s_cbranch_scc1 L_idle_%=\n"
      "s_add_u32 m0, s58, 0\n"
      "s_add_u32 s62, s56, 0\n"
      "buffer_load_dwordx4 v2, s[40:43], s62 offen offset:0 lds\n"
      "buffer_load_dwordx4 v2, s[40:43], s62 offen offset:1024 lds\n"
      "buffer_load_dwordx4 v2, s[40:43], s62 offen offset:2048 lds\n"
      "buffer_load_dwordx4 v3, s[40:43], s62 offen offset:3072 lds\n"
      "s_add_u32 s63, s60, 0\n"
      "buffer_load_dwordx4 a[0:3], v2, s[40:43], s63 offen offset:0\n"
      "buffer_load_dwordx4 a[4:7], v2, s[40:43], s63 offen offset:1024\n"
      "buffer_load_dwordx4 a[8:11], v2, s[40:43], s63 offen offset:2048\n"
      "buffer_load_dwordx4 a[12:15], v2, s[40:43], s63 offen offset:3072\n"
      "s_add_u32 s63, s60, 4096\n"
      "buffer_load_dwordx4 a[16:19], v2, s[40:43], s63 offen offset:0\n"
      "buffer_load_dwordx4 a[20:23], v2, s[40:43], s63 offen offset:1024\n"
      "buffer_load_dwordx4 a[24:27], v2, s[40:43], s63 offen offset:2048\n"
      "buffer_load_dwordx4 a[28:31], v2, s[40:43], s63 offen offset:3072\n"
      "s_add_u32 m0, s58, 16384\n"
      "s_add_u32 s62, s56, 16384\n"
      "buffer_load_dwordx4 v2, s[40:43], s62 offen offset:0 lds\n"
      "buffer_load_dwordx4 v2, s[40:43], s62 offen offset:1024 lds\n"
      "v_lshrrev_b32 v23, 4, v2\n"
      "v_and_b32 v24, 15, v23\n"
      "v_lshrrev_b32 v25, 4, v23\n"
      "v_bfe_u32 v26, v23, 1, 3\n"
      "v_lshlrev_b32 v27, 7, v24\n"
      "v_add_u32 v27, %[lds0], v27\n"
      "v_or_b32 v28, 0, v25\n"
      "v_xor_b32 v29, v28, v26\n"
      "v_lshl_add_u32 v10, v29, 4, v27\n"
      "v_add_u32 v12, 0x10000, v10\n"
      "v_or_b32 v28, 4, v25\n"
      "v_xor_b32 v29, v28, v26\n"
      "v_lshl_add_u32 v11, v29, 4, v27\n"
      "v_add_u32 v13, 0x10000, v11\n"
      "s_lshl_b32 s67, s52, 12\n"
      "s_add_u32 s67, s67, 131072\n"
      "v_mul_u32_u24 v28, 0xf800, v24\n"
      "v_lshl_add_u32 v5, v25, 4, v28\n"
      "v_cmp_lt_u32 vcc, 13, v24\n"
      "v_mov_b32 v31, 0x7fffff00\n"
      "s_nop 0\n"
      "v_cndmask_b32 v6, v5, v31, vcc\n"
      "v_and_b32 v24, 31, v23\n"
      "v_lshrrev_b32 v25, 5, v23\n"
      "s_add_u32 s67, s67, %[lds0]\n"
      "v_bfe_u32 v26, v23, 1, 2\n"
      "v_lshl_add_u32 v27, v24, 6, s67\n"
      "v_or_b32 v28, 0, v25\n"
      "v_xor_b32 v29, v28, v26\n"
      "v_lshl_add_u32 v14, v29, 4, v27\n"
      "v_or_b32 v28, 2, v25\n"
      "v_xor_b32 v29, v28, v26\n"
      "v_lshl_add_u32 v15, v29, 4, v27\n"
      "v_lshrrev_b32 v28, 2, v23\n"
      "v_bfe_u32 v29, v23, 3, 2\n"
      "v_and_b32 v30, 3, v23\n"
      "v_xor_b32 v29, v29, v30\n"
      "v_lshlrev_b32 v29, 4, v29\n"
      "v_lshl_add_u32 v29, v28, 6, v29\n"
      "v_add_u32 v7, s67, v29\n"
      "v_lshlrev_b32 v30, 4, v30\n"
      "v_lshl_add_u32 v8, v28, 10, v30\n"
      "v_mov_b32 v20, 1\n"
      "v_lshlrev_b32 v29, 4, v24\n"
      "v_lshl_add_u32 v21, v25, 13, v29\n"
      "v_cmp_eq_u32 vcc, 1, v25\n"
      "s_lshl_b32 s67, s52, 14\n"
      "s_add_u32 s57, s57, s67\n"
      "v_cndmask_b32 v22, v21, v31, vcc\n"
      "s_add_u32 s59, %[lds0], s67\n"
      "s_sub_u32 s79, s57, s67\n"
      "s_lshr_b32 s67, s52, 1\n"
      "s_lshl_b32 s67, s67, 14\n"
      "s_and_b32 s68, s52, 1\n"
      "s_lshl_b32 s68, s68, 11\n"
      "s_add_u32 s67, s67, s68\n"
      "s_add_u32 s79, s79, s67\n"
      "s_add_u32 s97, %[lds0], s67\n"
      "s_add_u32 s78, s79, s68\n"
      "s_add_u32 s96, s97, s68\n"
      "s_mul_i32 s61, s54, 7999488\n"
      "s_lshl_b32 s67, s53, 11\n"
      "s_add_u32 s61, s61, s67\n"
      "s_lshl_b32 s67, s55, 7\n"
      "s_add_u32 s61, s61, s67\n"
      "s_add_u32 s61, s61, 0x1740000\n"
      "s_lshl_b32 s68, s54, 16\n"
      "s_lshr_b32 s67, s55, 2\n"
      "s_lshl_b32 s67, s67, 5\n"
      "s_add_u32 s67, s53, s67\n"
      "s_lshl_b32 s67, s67, 7\n"
      "s_add_u32 s66, s67, s68\n"
      "s_add_u32 s66, s66, 0x26a2000\n"
      "v_cmp_eq_u32 vcc, 0, %[tid]\n"
      "s_nop 1\n"
      "v_cndmask_b32 v1, v31, 0, vcc\n"
      "s_lshl_b32 s67, s52, 15\n"
      "s_add_u32 s65, s67, 0x2682000\n"
      "s_lshl_b32 s67, s52, 11\n"
      "s_add_u32 s67, s67, 147456\n"
      "s_add_u32 s75, s67, %[lds0]\n"
      "s_getpc_b64 s[50:51]\n"
      "s_add_u32 s48, s50, wn_tab@rel32@lo+32772\n"
      "s_addc_u32 s49, s51, wn_tab@rel32@hi+32780\n"
      "s_and_b32 s49, s49, 0xffff\n"
      "s_mov_b32 s50, 8192\n"
      "s_mov_b32 s51, 0x20000\n"
      "s_mov_b32 s44, s38\n"
      "s_and_b32 s45, s39, 0xffff\n"
      "s_mov_b32 s46, 4096000\n"
      "s_mov_b32 s47, 0x20000\n"
      "s_cmp_ge_u32 %[bid], 126\n"
      "s_cselect_b32 s67, 1, 0\n"
      "s_mul_i32 s68, s67, 126\n"
      "s_sub_u32 s68, %[bid], s68\n"
      "s_lshr_b32 s69, s68, 5\n"
      "s_mov_b32 s74, s69\n"
      "s_lshl2_add_u32 s69, s67, s69\n"
      "s_lshl_b32 s69, s69, 14\n"
      "s_add_u32 s69, s69, 0x26a2000\n"
      "s_lshl_b32 s72, s52, 12\n"
      "s_add_u32 s69, s69, s72\n"
      "v_lshlrev_b32 v18, 3, v2\n"
      "v_min_u32 v18, 0xf00, v18\n"
      "s_lshr_b32 s72, s68, 1\n"
      "s_and_b32 s68, s68, 1\n"
      "s_mul_i32 s67, s67, 4000\n"
      "s_add_u32 s67, s67, s68\n"
      "s_lshl_b32 s68, s72, 6\n"
      "s_add_u32 s67, s67, s68\n"
      "s_lshl_b32 s67, s67, 9\n"
      "s_lshl_b32 s68, s52, 7\n"
      "s_add_u32 s71, s67, s68\n"
      "s_mul_i32 s70, %[bid], 63488\n"
      "s_lshl_b32 s68, s52, 9\n"
      "s_add_u32 s70, s70, s68\n"
      "s_add_u32 s70, s70, 0x1740000\n"
      "buffer_load_dwordx4 v2, s[40:43], s62 offen offset:2048 lds\n"
      "buffer_load_dwordx4 v3, s[40:43], s62 offen offset:3072 lds\n"
      "s_add_u32 m0, s58, 32768\n"
      "s_add_u32 s62, s56, 32768\n"
      "buffer_load_dwordx4 v2, s[40:43], s62 offen offset:0 lds\n"
      "buffer_load_dwordx4 v2, s[40:43], s62 offen offset:1024 lds\n"
      "s_waitcnt vmcnt(14)\n"
      "s_barrier\n"
      "ds_read_b128 v[32:35], v10 offset:0\n"
      "ds_read_b128 v[36:39], v11 offset:0\n"
      "ds_read_b128 v[40:43], v10 offset:2048\n"
      "ds_read_b128 v[44:47], v11 offset:2048\n"
      "s_waitcnt vmcnt(10)\n"
      "s_waitcnt lgkmcnt(3)\n"
      "v_mfma_f32_16x16x32_bf16 v[64:67], a[0:3], v[32:35], 0\n"
      "ds_read_b128 v[48:51], v10 offset:4096\n"
      "s_waitcnt vmcnt(6)\n"
      "s_waitcnt lgkmcnt(3)\n"
      "v_mfma_f32_16x16x32_bf16 v[64:67], a[16:19], v[36:39], v[64:67]\n"
      "buffer_load_dwordx4 v2, s[40:43], s62 offen offset:2048 lds\n"
      "v_mfma_f32_16x16x32_bf16 v[68:71], a[24:27], v[36:39], 0\n"
      "s_nop 3\n"
      "v_mfma_f32_16x16x32_bf16 v[68:71], a[8:11], v[32:35], v[68:71]\n"
      "v_mfma_f32_16x16x32_bf16 v[84:87], a[12:15], v[32:35], 0\n"
      "ds_read_b128 v[52:55], v11 offset:4096\n"
      "v_mfma_f32_16x16x32_bf16 v[84:87], a[28:31], v[36:39], v[84:87]\n"
      "v_mfma_f32_16x16x32_bf16 v[80:83], a[20:23], v[36:39], 0\n"
      "s_nop 3\n"
      "v_mfma_f32_16x16x32_bf16 v[80:83], a[4:7], v[32:35], v[80:83]\n"
      "s_waitcnt lgkmcnt(3)\n"
      "v_mfma_f32_16x16x32_bf16 v[88:91], a[0:3], v[40:43], 0\n"
      "ds_read_b128 v[56:59], v10 offset:6144\n"
      "s_waitcnt lgkmcnt(3)\n"
      "v_mfma_f32_16x16x32_bf16 v[88:91], a[16:19], v[44:47], v[88:91]\n"
      "buffer_load_dwordx4 v3, s[40:43], s62 offen offset:3072 lds\n"
      "v_mfma_f32_16x16x32_bf16 v[92:95], a[24:27], v[44:47], 0\n"
      "s_nop 3\n"
      "v_mfma_f32_16x16x32_bf16 v[92:95], a[8:11], v[40:43], v[92:95]\n"
      "v_mfma_f32_16x16x32_bf16 v[108:111], a[12:15], v[40:43], 0\n"
      "ds_read_b128 v[60:63], v11 offset:6144\n"
      "v_mfma_f32_16x16x32_bf16 v[108:111], a[28:31], v[44:47], v[108:111]\n"
      "s_add_u32 s63, s60, 8192\n"
      "buffer_load_dwordx4 a[32:35], v2, s[40:43], s63 offen offset:0\n"
      "v_mfma_f32_16x16x32_bf16 v[104:107], a[20:23], v[44:47], 0\n"
      "s_nop 3\n"
      "v_mfma_f32_16x16x32_bf16 v[104:107], a[4:7], v[40:43], v[104:107]\n"
      "s_waitcnt lgkmcnt(3)\n"
      "v_mfma_f32_16x16x32_bf16 v[112:115], a[0:3], v[48:51], 0\n"
      "ds_read_b128 v[32:35], v10 offset:8192\n"
      "s_waitcnt lgkmcnt(3)\n"
      "v_mfma_f32_16x16x32_bf16 v[112:115], a[16:19], v[52:55], v[112:115]\n"
      "v_mfma_f32_16x16x32_bf16 v[116:119], a[24:27], v[52:55], 0\n"
      "s_nop 3\n"
      "v_mfma_f32_16x16x32_bf16 v[116:119], a[8:11], v[48:51], v[116:119]\n"
      "v_mfma_f32_16x16x32_bf16 v[132:135], a[12:15], v[48:51], 0\n"
      "ds_read_b128 v[36:39], v11 offset:8192\n"
      "v_mfma_f32_16x16x32_bf16 v[132:135], a[28:31], v[52:55], v[132:135]\n"
      "buffer_load_dwordx4 a[36:39], v2, s[40:43], s63 offen offset:1024\n"
      "v_mfma_f32_16x16x32_bf16 v[128:131], a[20:23], v[52:55], 0\n"
      "s_nop 3\n"
      "v_mfma_f32_16x16x32_bf16 v[128:131], a[4:7], v[48:51], v[128:131]\n"
      "s_waitcnt lgkmcnt(3)\n"
      "v_mfma_f32_16x16x32_bf16 v[136:139], a[0:3], v[56:59], 0\n"
      "ds_read_b128 v[40:43], v10 offset:10240\n"
      "s_waitcnt lgkmcnt(3)\n"
      "v_mfma_f32_16x16x32_bf16 v[136:139], a[16:19], v[60:63], v[136:139]\n"
      "buffer_load_dwordx4 a[40:43], v2, s[40:43], s63 offen offset:2048\n"
      "v_mfma_f32_16x16x32_bf16 v[140:143], a[24:27], v[60:63], 0\n"
      "s_nop 3\n"
      "v_mfma_f32_16x16x32_bf16 v[140:143], a[8:11], v[56:59], v[140:143]\n"
      "v_mfma_f32_16x16x32_bf16 v[156:159], a[12:15], v[56:59], 0\n"
      "ds_read_b128 v[44:47], v11 offset:10240\n"
      "v_mfma_f32_16x16x32_bf16 v[156:159], a[28:31], v[60:63], v[156:159]\n"
      "v_mfma_f32_16x16x32_bf16 v[152:155], a[20:23], v[60:63], 0\n"
      "s_nop 3\n"
      "v_mfma_f32_16x16x32_bf16 v[152:155], a[4:7], v[56:59], v[152:155]\n"
      "s_waitcnt lgkmcnt(3)\n"
      "v_mfma_f32_16x16x32_bf16 v[160:163], a[0:3], v[32:35], 0\n"
      "ds_read_b128 v[48:51], v10 offset:12288\n"
      "s_waitcnt lgkmcnt(3)\n"
      "v_mfma_f32_16x16x32_bf16 v[160:163], a[16:19], v[36:39], v[160:163]\n"
      "buffer_load_dwordx4 a[44:47], v2, s[40:43], s63 offen offset:3072\n"
      "v_mfma_f32_16x16x32_bf16 v[164:167], a[24:27], v[36:39], 0\n"
      "s_nop 3\n"
      "v_mfma_f32_16x16x32_bf16 v[164:167], a[8:11], v[32:35], v[164:167]\n"
      "v_mfma_f32_16x16x32_bf16 v[180:183], a[12:15], v[32:35], 0\n"
      "ds_read_b128 v[52:55], v11 offset:12288\n"
      "v_mfma_f32_16x16x32_bf16 v[180:183], a[28:31], v[36:39], v[180:183]\n"
      "s_add_u32 s63, s60, 12288\n"
      "buffer_load_dwordx4 a[48:51], v2, s[40:43], s63 offen offset:0\n"
      "v_mfma_f32_16x16x32_bf16 v[176:179], a[20:23], v[36:39], 0\n"
      "s_nop 3\n"
      "v_mfma_f32_16x16x32_bf16 v[176:179], a[4:7], v[32:35], v[176:179]\n"
      "s_waitcnt lgkmcnt(3)\n"
      "v_mfma_f32_16x16x32_bf16 v[184:187], a[0:3], v[40:43], 0\n"
      "ds_read_b128 v[56:59], v10 offset:14336\n"
      "s_waitcnt lgkmcnt(3)\n"
      "v_mfma_f32_16x16x32_bf16 v[184:187], a[16:19], v[44:47], v[184:187]\n"
      "v_mfma_f32_16x16x32_bf16 v[188:191], a[24:27], v[44:47], 0\n"
      "s_nop 3\n"
      "v_mfma_f32_16x16x32_bf16 v[188:191], a[8:11], v[40:43], v[188:191]\n"
      "v_mfma_f32_16x16x32_bf16 v[204:207], a[12:15], v[40:43], 0\n"
      "ds_read_b128 v[60:63], v11 offset:14336\n"
      "v_mfma_f32_16x16x32_bf16 v[204:207], a[28:31], v[44:47], v[204:207]\n"
      "buffer_load_dwordx4 a[52:55], v2, s[40:43], s63 offen offset:1024\n"
      "v_mfma_f32_16x16x32_bf16 v[200:203], a[20:23], v[44:47], 0\n"
      "s_nop 3\n"
      "v_mfma_f32_16x16x32_bf16 v[200:203], a[4:7], v[40:43], v[200:203]\n"
      "s_waitcnt lgkmcnt(3)\n"
      "v_mfma_f32_16x16x32_bf16 v[208:211], a[0:3], v[48:51], 0\n"
      "s_waitcnt vmcnt(10)\n"
      "s_barrier\n"
      "ds_read_b128 v[32:35], v10 offset:16384\n"
      "s_waitcnt lgkmcnt(3)\n"
      "v_mfma_f32_16x16x32_bf16 v[208:211], a[16:19], v[52:55], v[208:211]\n"
      "buffer_load_dwordx4 a[56:59], v2, s[40:43], s63 offen offset:2048\n"
      "v_mfma_f32_16x16x32_bf16 v[212:215], a[24:27], v[52:55], 0\n"
      "s_nop 3\n"
      "v_mfma_f32_16x16x32_bf16 v[212:215], a[8:11], v[48:51], v[212:215]\n"
      "v_mfma_f32_16x16x32_bf16 v[228:231], a[12:15], v[48:51], 0\n"
      "ds_read_b128 v[36:39], v11 offset:16384\n"
      "v_mfma_f32_16x16x32_bf16 v[228:231], a[28:31], v[52:55], v[228:231]\n"
      "v_mfma_f32_16x16x32_bf16 v[224:227], a[20:23], v[52:55], 0\n"
      "s_nop 3\n"
      "v_mfma_f32_16x16x32_bf16 v[224:227], a[4:7], v[48:51], v[224:227]\n"
      "s_waitcnt lgkmcnt(3)\n"
      "v_mfma_f32_16x16x32_bf16 v[232:235], a[0:3], v[56:59], 0\n"
      "ds_read_b128 v[40:43], v10 offset:18432\n"
      "s_waitcnt lgkmcnt(3)\n"
      "v_mfma_f32_16x16x32_bf16 v[232:235], a[16:19], v[60:63], v[232:235]\n"
      "buffer_load_dwordx4 a[60:63], v2, s[40:43], s63 offen offset:3072\n"
      "v_mfma_f32_16x16x32_bf16 v[236:239], a[24:27], v[60:63], 0\n"
      "s_nop 3\n"
      "v_mfma_f32_16x16x32_bf16 v[236:239], a[8:11], v[56:59], v[236:239]\n"
      "v_mfma_f32_16x16x32_bf16 v[252:255], a[12:15], v[56:59], 0\n"
      "ds_read_b128 v[44:47], v11 offset:18432\n"
      "v_mfma_f32_16x16x32_bf16 v[252:255], a[28:31], v[60:63], v[252:255]\n"
      "s_add_u32 m0, s58, 49152\n"
      "s_add_u32 s62, s56, 49152\n"
      "buffer_load_dwordx4 v2, s[40:43], s62 offen offset:0 lds\n"
      "v_mfma_f32_16x16x32_bf16 v[248:251], a[20:23], v[60:63], 0\n"
      "s_nop 3\n"
      "v_mfma_f32_16x16x32_bf16 v[248:251], a[4:7], v[56:59], v[248:251]\n"
      "s_waitcnt lgkmcnt(3)\n"
      "v_mfma_f32_16x16x32_bf16 v[80:83], a[0:3], v[32:35], v[80:83]\n"
      "ds_read_b128 v[48:51], v10 offset:20480\n"
      "s_waitcnt lgkmcnt(3)\n"
      "v_mfma_f32_16x16x32_bf16 v[80:83], a[16:19], v[36:39], v[80:83]\n"
      "v_mfma_f32_16x16x32_bf16 v[84:87], a[24:27], v[36:39], v[84:87]\n"
      "s_nop 3\n"
      "v_mfma_f32_16x16x32_bf16 v[84:87], a[8:11], v[32:35], v[84:87]\n"
      "v_mfma_f32_16x16x32_bf16 v[76:79], a[12:15], v[32:35], 0\n"
      "ds_read_b128 v[52:55], v11 offset:20480\n"
      "v_mfma_f32_16x16x32_bf16 v[76:79], a[28:31], v[36:39], v[76:79]\n"
      "buffer_load_dwordx4 v2, s[40:43], s62 offen offset:1024 lds\n"
      "v_mfma_f32_16x16x32_bf16 v[72:75], a[20:23], v[36:39], 0\n"
      "s_nop 3\n"
      "v_mfma_f32_16x16x32_bf16 v[72:75], a[4:7], v[32:35], v[72:75]\n"
      "s_waitcnt lgkmcnt(3)\n"
      "v_mfma_f32_16x16x32_bf16 v[104:107], a[0:3], v[40:43], v[104:107]\n"
      "ds_read_b128 v[56:59], v10 offset:22528\n"
      "s_waitcnt lgkmcnt(3)\n"
      "v_mfma_f32_16x16x32_bf16 v[104:107], a[16:19], v[44:47], v[104:107]\n"
      "buffer_load_dwordx4 v2, s[40:43], s62 offen offset:2048 lds\n"
      "v_mfma_f32_16x16x32_bf16 v[108:111], a[24:27], v[44:47], v[108:111]\n"
      "s_nop 3\n"
      "v_mfma_f32_16x16x32_bf16 v[108:111], a[8:11], v[40:43], v[108:111]\n"
      "v_mfma_f32_16x16x32_bf16 v[100:103], a[12:15], v[40:43], 0\n"
      "ds_read_b128 v[60:63], v11 offset:22528\n"
      "v_mfma_f32_16x16x32_bf16 v[100:103], a[28:31], v[44:47], v[100:103]\n"
      "v_mfma_f32_16x16x32_bf16 v[96:99], a[20:23], v[44:47], 0\n"
      "s_nop 3\n"
      "v_mfma_f32_16x16x32_bf16 v[96:99], a[4:7], v[40:43], v[96:99]\n"
      "s_waitcnt lgkmcnt(3)\n"
      "v_mfma_f32_16x16x32_bf16 v[128:131], a[0:3], v[48:51], v[128:131]\n"
      "ds_read_b128 v[32:35], v10 offset:24576\n"
      "s_waitcnt lgkmcnt(3)\n"
      "v_mfma_f32_16x16x32_bf16 v[128:131], a[16:19], v[52:55], v[128:131]\n"
      "buffer_load_dwordx4 v3, s[40:43], s62 offen offset:3072 lds\n"
      "v_mfma_f32_16x16x32_bf16 v[132:135], a[24:27], v[52:55], v[132:135]\n"
      "s_nop 3\n"
      "v_mfma_f32_16x16x32_bf16 v[132:135], a[8:11], v[48:51], v[132:135]\n"
      "v_mfma_f32_16x16x32_bf16 v[124:127], a[12:15], v[48:51], 0\n"
      "ds_read_b128 v[36:39], v11 offset:24576\n"
      "v_mfma_f32_16x16x32_bf16 v[124:127], a[28:31], v[52:55], v[124:127]\n"
      "s_add_u32 m0, s59, 65536\n"
      "s_add_u32 s62, s57, 65536\n"
      "buffer_load_dwordx4 v2, s[40:43], s62 offen offset:0 lds\n"
      "v_mfma_f32_16x16x32_bf16 v[120:123], a[20:23], v[52:55], 0\n"
      "s_nop 3\n"
      "v_mfma_f32_16x16x32_bf16 v[120:123], a[4:7], v[48:51], v[120:123]\n"
      "s_waitcnt lgkmcnt(3)\n"
      "v_mfma_f32_16x16x32_bf16 v[152:155], a[0:3], v[56:59], v[152:155]\n"
      "ds_read_b128 v[40:43], v10 offset:26624\n"
      "s_waitcnt lgkmcnt(3)\n"
      "v_mfma_f32_16x16x32_bf16 v[152:155], a[16:19], v[60:63], v[152:155]\n"
      "v_mfma_f32_16x16x32_bf16 v[156:159], a[24:27], v[60:63], v[156:159]\n"
      "s_nop 3\n"
      "v_mfma_f32_16x16x32_bf16 v[156:159], a[8:11], v[56:59], v[156:159]\n"
      "v_mfma_f32_16x16x32_bf16 v[148:151], a[12:15], v[56:59], 0\n"
      "ds_read_b128 v[44:47], v11 offset:26624\n"
      "v_mfma_f32_16x16x32_bf16 v[148:151], a[28:31], v[60:63], v[148:151]\n"
      "buffer_load_dwordx4 v2, s[40:43], s62 offen offset:1024 lds\n"
      "v_mfma_f32_16x16x32_bf16 v[144:147], a[20:23], v[60:63], 0\n"
      "s_nop 3\n"
      "v_mfma_f32_16x16x32_bf16 v[144:147], a[4:7], v[56:59], v[144:147]\n"
      "s_waitcnt lgkmcnt(3)\n"
      "v_mfma_f32_16x16x32_bf16 v[176:179], a[0:3], v[32:35], v[176:179]\n"
      "ds_read_b128 v[48:51], v10 offset:28672\n"
      "s_waitcnt lgkmcnt(3)\n"
      "v_mfma_f32_16x16x32_bf16 v[176:179], a[16:19], v[36:39], v[176:179]\n"
      "buffer_load_dwordx4 v2, s[40:43], s62 offen offset:2048 lds\n"
      "v_mfma_f32_16x16x32_bf16 v[180:183], a[24:27], v[36:39], v[180:183]\n"
      "s_nop 3\n"
      "v_mfma_f32_16x16x32_bf16 v[180:183], a[8:11], v[32:35], v[180:183]\n"
      "v_mfma_f32_16x16x32_bf16 v[172:175], a[12:15], v[32:35], 0\n"
      "ds_read_b128 v[52:55], v11 offset:28672\n"
      "v_mfma_f32_16x16x32_bf16 v[172:175], a[28:31], v[36:39], v[172:175]\n"
      "v_mfma_f32_16x16x32_bf16 v[168:171], a[20:23], v[36:39], 0\n"
      "s_nop 3\n"
      "v_mfma_f32_16x16x32_bf16 v[168:171], a[4:7], v[32:35], v[168:171]\n"
      "s_waitcnt lgkmcnt(3)\n"
      "v_mfma_f32_16x16x32_bf16 v[200:203], a[0:3], v[40:43], v[200:203]\n"
      "ds_read_b128 v[56:59], v10 offset:30720\n"
      "s_waitcnt lgkmcnt(3)\n"
      "v_mfma_f32_16x16x32_bf16 v[200:203], a[16:19], v[44:47], v[200:203]\n"
      "buffer_load_dwordx4 v2, s[40:43], s62 offen offset:3072 lds\n"
      "v_mfma_f32_16x16x32_bf16 v[204:207], a[24:27], v[44:47], v[204:207]\n"
      "s_nop 3\n"
      "v_mfma_f32_16x16x32_bf16 v[204:207], a[8:11], v[40:43], v[204:207]\n"
      "v_mfma_f32_16x16x32_bf16 v[196:199], a[12:15], v[40:43], 0\n"
      "ds_read_b128 v[60:63], v11 offset:30720\n"
      "v_mfma_f32_16x16x32_bf16 v[196:199], a[28:31], v[44:47], v[196:199]\n"
      "s_add_u32 s63, s60, 16384\n"
      "buffer_load_dwordx4 a[64:67], v2, s[40:43], s63 offen offset:0\n"
      "v_mfma_f32_16x16x32_bf16 v[192:195], a[20:23], v[44:47], 0\n"
      "s_nop 3\n"
      "v_mfma_f32_16x16x32_bf16 v[192:195], a[4:7], v[40:43], v[192:195]\n"
      "s_waitcnt lgkmcnt(3)\n"
      "v_mfma_f32_16x16x32_bf16 v[224:227], a[0:3], v[48:51], v[224:227]\n"
      "s_waitcnt vmcnt(17)\n"
      "s_barrier\n"
      "ds_read_b128 v[32:35], v10 offset:32768\n"
      "s_waitcnt lgkmcnt(3)\n"
      "v_mfma_f32_16x16x32_bf16 v[224:227], a[16:19], v[52:55], v[224:227]\n"
      "v_mfma_f32_16x16x32_bf16 v[228:231], a[24:27], v[52:55], v[228:231]\n"
      "s_nop 3\n"
      "v_mfma_f32_16x16x32_bf16 v[228:231], a[8:11], v[48:51], v[228:231]\n"
      "v_mfma_f32_16x16x32_bf16 v[220:223], a[12:15], v[48:51], 0\n"
      "ds_read_b128 v[36:39], v11 offset:32768\n"
      "v_mfma_f32_16x16x32_bf16 v[220:223], a[28:31], v[52:55], v[220:223]\n"
      "buffer_load_dwordx4 a[68:71], v2, s[40:43], s63 offen offset:1024\n"
      "v_mfma_f32_16x16x32_bf16 v[216:219], a[20:23], v[52:55], 0\n"
      "s_nop 3\n"
      "v_mfma_f32_16x16x32_bf16 v[216:219], a[4:7], v[48:51], v[216:219]\n"
      "s_waitcnt lgkmcnt(3)\n"
      "v_mfma_f32_16x16x32_bf16 v[248:251], a[0:3], v[56:59], v[248:251]\n"
      "ds_read_b128 v[40:43], v10 offset:34816\n"
      "s_waitcnt lgkmcnt(3)\n"
      "v_mfma_f32_16x16x32_bf16 v[248:251], a[16:19], v[60:63], v[248:251]\n"
      "buffer_load_dwordx4 a[72:75], v2, s[40:43], s63 offen offset:2048\n"
      "v_mfma_f32_16x16x32_bf16 v[252:255], a[24:27], v[60:63], v[252:255]\n"
      "s_nop 3\n"
      "v_mfma_f32_16x16x32_bf16 v[252:255], a[8:11], v[56:59], v[252:255]\n"
      "v_mfma_f32_16x16x32_bf16 v[244:247], a[12:15], v[56:59], 0\n"
      "ds_read_b128 v[44:47], v11 offset:34816\n"
      "v_mfma_f32_16x16x32_bf16 v[244:247], a[28:31], v[60:63], v[244:247]\n"
      "v_mfma_f32_16x16x32_bf16 v[240:243], a[20:23], v[60:63], 0\n"
      "s_nop 3\n"
      "v_mfma_f32_16x16x32_bf16 v[240:243], a[4:7], v[56:59], v[240:243]\n"
      "s_waitcnt vmcnt(15)\n"
      "s_waitcnt lgkmcnt(3)\n"
      "v_mfma_f32_16x16x32_bf16 v[64:67], a[32:35], v[32:35], v[64:67]\n"
      "ds_read_b128 v[48:51], v10 offset:36864\n"
      "s_waitcnt vmcnt(11)\n"
      "s_waitcnt lgkmcnt(3)\n"
      "v_mfma_f32_16x16x32_bf16 v[64:67], a[48:51], v[36:39], v[64:67]\n"
      "buffer_load_dwordx4 a[76:79], v2, s[40:43], s63 offen offset:3072\n"
      "v_mfma_f32_16x16x32_bf16 v[68:71], a[56:59], v[36:39], v[68:71]\n"
      "s_nop 3\n"
      "v_mfma_f32_16x16x32_bf16 v[68:71], a[40:43], v[32:35], v[68:71]\n"
      "v_mfma_f32_16x16x32_bf16 v[84:87], a[44:47], v[32:35], v[84:87]\n"
      "ds_read_b128 v[52:55], v11 offset:36864\n"
      "v_mfma_f32_16x16x32_bf16 v[84:87], a[60:63], v[36:39], v[84:87]\n"
      "s_add_u32 s63, s60, 20480\n"
      "buffer_load_dwordx4 a[80:83], v2, s[40:43], s63 offen offset:0\n"
      "v_mfma_f32_16x16x32_bf16 v[80:83], a[52:55], v[36:39], v[80:83]\n"
      "s_nop 3\n"
      "v_mfma_f32_16x16x32_bf16 v[80:83], a[36:39], v[32:35], v[80:83]\n"
      "s_waitcnt lgkmcnt(3)\n"
      "v_mfma_f32_16x16x32_bf16 v[88:91], a[32:35], v[40:43], v[88:91]\n"
      "ds_read_b128 v[56:59], v10 offset:38912\n"
      "s_waitcnt lgkmcnt(3)\n"
      "v_mfma_f32_16x16x32_bf16 v[88:91], a[48:51], v[44:47], v[88:91]\n"
      "v_mfma_f32_16x16x32_bf16 v[92:95], a[56:59], v[44:47], v[92:95]\n"
      "s_nop 3\n"
      "v_mfma_f32_16x16x32_bf16 v[92:95], a[40:43], v[40:43], v[92:95]\n"
      "v_mfma_f32_16x16x32_bf16 v[108:111], a[44:47], v[40:43], v[108:111]\n"
      "ds_read_b128 v[60:63], v11 offset:38912\n"
      "v_mfma_f32_16x16x32_bf16 v[108:111], a[60:63], v[44:47], v[108:111]\n"
      "buffer_load_dwordx4 a[84:87], v2, s[40:43], s63 offen offset:1024\n"
      "v_mfma_f32_16x16x32_bf16 v[104:107], a[52:55], v[44:47], v[104:107]\n"
      "s_nop 3\n"
      "v_mfma_f32_16x16x32_bf16 v[104:107], a[36:39], v[40:43], v[104:107]\n"
      "s_waitcnt lgkmcnt(3)\n"
      "v_mfma_f32_16x16x32_bf16 v[112:115], a[32:35], v[48:51], v[112:115]\n"
      "ds_read_b128 v[32:35], v10 offset:40960\n"
      "s_waitcnt lgkmcnt(3)\n"
      "v_mfma_f32_16x16x32_bf16 v[112:115], a[48:51], v[52:55], v[112:115]\n"
      "buffer_load_dwordx4 a[88:91], v2, s[40:43], s63 offen offset:2048\n"
      "v_mfma_f32_16x16x32_bf16 v[116:119], a[56:59], v[52:55], v[116:119]\n"
      "s_nop 3\n"
      "v_mfma_f32_16x16x32_bf16 v[116:119], a[40:43], v[48:51], v[116:119]\n"
      "v_mfma_f32_16x16x32_bf16 v[132:135], a[44:47], v[48:51], v[132:135]\n"
      "ds_read_b128 v[36:39], v11 offset:40960\n"
      "v_mfma_f32_16x16x32_bf16 v[132:135], a[60:63], v[52:55], v[132:135]\n"
      "v_mfma_f32_16x16x32_bf16 v[128:131], a[52:55], v[52:55], v[128:131]\n"
      "s_nop 3\n"
      "v_mfma_f32_16x16x32_bf16 v[128:131], a[36:39], v[48:51], v[128:131]\n"
      "s_waitcnt lgkmcnt(3)\n"
      "v_mfma_f32_16x16x32_bf16 v[136:139], a[32:35], v[56:59], v[136:139]\n"
      "ds_read_b128 v[40:43], v10 offset:43008\n"
      "s_waitcnt lgkmcnt(3)\n"
      "v_mfma_f32_16x16x32_bf16 v[136:139], a[48:51], v[60:63], v[136:139]\n"
      "buffer_load_dwordx4 a[92:95], v2, s[40:43], s63 offen offset:3072\n"
      "v_mfma_f32_16x16x32_bf16 v[140:143], a[56:59], v[60:63], v[140:143]\n"
      "s_nop 3\n"
      "v_mfma_f32_16x16x32_bf16 v[140:143], a[40:43], v[56:59], v[140:143]\n"
      "v_mfma_f32_16x16x32_bf16 v[156:159], a[44:47], v[56:59], v[156:159]\n"
      "ds_read_b128 v[44:47], v11 offset:43008\n"
      "v_mfma_f32_16x16x32_bf16 v[156:159], a[60:63], v[60:63], v[156:159]\n"
      "s_add_u32 s63, s60, 24576\n"
      "buffer_load_dwordx4 a[96:99], v2, s[40:43], s63 offen offset:0\n"
      "v_mfma_f32_16x16x32_bf16 v[152:155], a[52:55], v[60:63], v[152:155]\n"
      "s_nop 3\n"
      "v_mfma_f32_16x16x32_bf16 v[152:155], a[36:39], v[56:59], v[152:155]\n"
      "s_waitcnt lgkmcnt(3)\n"
      "v_mfma_f32_16x16x32_bf16 v[160:163], a[32:35], v[32:35], v[160:163]\n"
      "ds_read_b128 v[48:51], v10 offset:45056\n"
      "s_waitcnt lgkmcnt(3)\n"
      "v_mfma_f32_16x16x32_bf16 v[160:163], a[48:51], v[36:39], v[160:163]\n"
      "v_mfma_f32_16x16x32_bf16 v[164:167], a[56:59], v[36:39], v[164:167]\n"
      "s_nop 3\n"
      "v_mfma_f32_16x16x32_bf16 v[164:167], a[40:43], v[32:35], v[164:167]\n"
      "v_mfma_f32_16x16x32_bf16 v[180:183], a[44:47], v[32:35], v[180:183]\n"
      "ds_read_b128 v[52:55], v11 offset:45056\n"
      "v_mfma_f32_16x16x32_bf16 v[180:183], a[60:63], v[36:39], v[180:183]\n"
      "buffer_load_dwordx4 a[100:103], v2, s[40:43], s63 offen offset:1024\n"
      "v_mfma_f32_16x16x32_bf16 v[176:179], a[52:55], v[36:39], v[176:179]\n"
      "s_nop 3\n"
      "v_mfma_f32_16x16x32_bf16 v[176:179], a[36:39], v[32:35], v[176:179]\n"
      "s_waitcnt lgkmcnt(3)\n"
      "v_mfma_f32_16x16x32_bf16 v[184:187], a[32:35], v[40:43], v[184:187]\n"
      "ds_read_b128 v[56:59], v10 offset:47104\n"
      "s_waitcnt lgkmcnt(3)\n"
      "v_mfma_f32_16x16x32_bf16 v[184:187], a[48:51], v[44:47], v[184:187]\n"
      "buffer_load_dwordx4 a[104:107], v2, s[40:43], s63 offen offset:2048\n"
      "v_mfma_f32_16x16x32_bf16 v[188:191], a[56:59], v[44:47], v[188:191]\n"
      "s_nop 3\n"
      "v_mfma_f32_16x16x32_bf16 v[188:191], a[40:43], v[40:43], v[188:191]\n"
      "v_mfma_f32_16x16x32_bf16 v[204:207], a[44:47], v[40:43], v[204:207]\n"
      "ds_read_b128 v[60:63], v11 offset:47104\n"
      "v_mfma_f32_16x16x32_bf16 v[204:207], a[60:63], v[44:47], v[204:207]\n"
      "v_mfma_f32_16x16x32_bf16 v[200:203], a[52:55], v[44:47], v[200:203]\n"
      "s_nop 3\n"
      "v_mfma_f32_16x16x32_bf16 v[200:203], a[36:39], v[40:43], v[200:203]\n"
      "s_waitcnt lgkmcnt(3)\n"
      "v_mfma_f32_16x16x32_bf16 v[208:211], a[32:35], v[48:51], v[208:211]\n"
      "s_waitcnt vmcnt(15)\n"
      "s_barrier\n"
      "ds_read_b128 v[32:35], v10 offset:49152\n"
      "s_waitcnt lgkmcnt(3)\n"
      "v_mfma_f32_16x16x32_bf16 v[208:211], a[48:51], v[52:55], v[208:211]\n"
      "buffer_load_dwordx4 a[108:111], v2, s[40:43], s63 offen offset:3072\n"
      "v_mfma_f32_16x16x32_bf16 v[212:215], a[56:59], v[52:55], v[212:215]\n"
      "s_nop 3\n"
      "v_mfma_f32_16x16x32_bf16 v[212:215], a[40:43], v[48:51], v[212:215]\n"
      "v_mfma_f32_16x16x32_bf16 v[228:231], a[44:47], v[48:51], v[228:231]\n"
      "ds_read_b128 v[36:39], v11 offset:49152\n"
      "v_mfma_f32_16x16x32_bf16 v[228:231], a[60:63], v[52:55], v[228:231]\n"
      "s_add_u32 s63, s60, 28672\n"
      "buffer_load_dwordx4 a[112:115], v2, s[40:43], s63 offen offset:0\n"
      "v_mfma_f32_16x16x32_bf16 v[224:227], a[52:55], v[52:55], v[224:227]\n"
      "s_nop 3\n"
      "v_mfma_f32_16x16x32_bf16 v[224:227], a[36:39], v[48:51], v[224:227]\n"
      "s_waitcnt lgkmcnt(3)\n"
      "v_mfma_f32_16x16x32_bf16 v[232:235], a[32:35], v[56:59], v[232:235]\n"
      "ds_read_b128 v[40:43], v10 offset:51200\n"
      "s_waitcnt lgkmcnt(3)\n"
      "v_mfma_f32_16x16x32_bf16 v[232:235], a[48:51], v[60:63], v[232:235]\n"
      "v_mfma_f32_16x16x32_bf16 v[236:239], a[56:59], v[60:63], v[236:239]\n"
      "s_nop 3\n"
      "v_mfma_f32_16x16x32_bf16 v[236:239], a[40:43], v[56:59], v[236:239]\n"
      "v_mfma_f32_16x16x32_bf16 v[252:255], a[44:47], v[56:59], v[252:255]\n"
      "ds_read_b128 v[44:47], v11 offset:51200\n"
      "v_mfma_f32_16x16x32_bf16 v[252:255], a[60:63], v[60:63], v[252:255]\n"
      "buffer_load_dwordx4 a[116:119], v2, s[40:43], s63 offen offset:1024\n"
      "v_mfma_f32_16x16x32_bf16 v[248:251], a[52:55], v[60:63], v[248:251]\n"
      "s_nop 3\n"
      "v_mfma_f32_16x16x32_bf16 v[248:251], a[36:39], v[56:59], v[248:251]\n"
      "s_waitcnt lgkmcnt(3)\n"
      "v_mfma_f32_16x16x32_bf16 v[80:83], a[32:35], v[32:35], v[80:83]\n"
      "ds_read_b128 v[48:51], v10 offset:53248\n"
      "s_waitcnt lgkmcnt(3)\n"
      "v_mfma_f32_16x16x32_bf16 v[80:83], a[48:51], v[36:39], v[80:83]\n"
      "buffer_load_dwordx4 a[120:123], v2, s[40:43], s63 offen offset:2048\n"
      "v_mfma_f32_16x16x32_bf16 v[84:87], a[56:59], v[36:39], v[84:87]\n"
      "s_nop 3\n"
      "v_mfma_f32_16x16x32_bf16 v[84:87], a[40:43], v[32:35], v[84:87]\n"
      "v_mfma_f32_16x16x32_bf16 v[76:79], a[44:47], v[32:35], v[76:79]\n"
      "ds_read_b128 v[52:55], v11 offset:53248\n"
      "v_mfma_f32_16x16x32_bf16 v[76:79], a[60:63], v[36:39], v[76:79]\n"
      "v_mfma_f32_16x16x32_bf16 v[72:75], a[52:55], v[36:39], v[72:75]\n"
      "s_nop 3\n"
      "v_mfma_f32_16x16x32_bf16 v[72:75], a[36:39], v[32:35], v[72:75]\n"
      "s_waitcnt lgkmcnt(3)\n"
      "v_mfma_f32_16x16x32_bf16 v[104:107], a[32:35], v[40:43], v[104:107]\n"
      "ds_read_b128 v[56:59], v10 offset:55296\n"
      "s_waitcnt lgkmcnt(3)\n"
      "v_mfma_f32_16x16x32_bf16 v[104:107], a[48:51], v[44:47], v[104:107]\n"
      "buffer_load_dwordx4 a[124:127], v2, s[40:43], s63 offen offset:3072\n"
      "v_mfma_f32_16x16x32_bf16 v[108:111], a[56:59], v[44:47], v[108:111]\n"
      "s_nop 3\n"
      "v_mfma_f32_16x16x32_bf16 v[108:111], a[40:43], v[40:43], v[108:111]\n"
      "v_mfma_f32_16x16x32_bf16 v[100:103], a[44:47], v[40:43], v[100:103]\n"
      "ds_read_b128 v[60:63], v11 offset:55296\n"
      "v_mfma_f32_16x16x32_bf16 v[100:103], a[60:63], v[44:47], v[100:103]\n"
      "s_add_u32 m0, s59, 69632\n"
      "s_add_u32 s62, s57, 69632\n"
      "buffer_load_dwordx4 v2, s[40:43], s62 offen offset:0 lds\n"
      "v_mfma_f32_16x16x32_bf16 v[96:99], a[52:55], v[44:47], v[96:99]\n"
      "s_nop 3\n"
      "v_mfma_f32_16x16x32_bf16 v[96:99], a[36:39], v[40:43], v[96:99]\n"
      "s_waitcnt lgkmcnt(3)\n"
      "v_mfma_f32_16x16x32_bf16 v[128:131], a[32:35], v[48:51], v[128:131]\n"
      "ds_read_b128 v[32:35], v10 offset:57344\n"
      "s_waitcnt lgkmcnt(3)\n"
      "v_mfma_f32_16x16x32_bf16 v[128:131], a[48:51], v[52:55], v[128:131]\n"
      "v_mfma_f32_16x16x32_bf16 v[132:135], a[56:59], v[52:55], v[132:135]\n"
      "s_nop 3\n"
      "v_mfma_f32_16x16x32_bf16 v[132:135], a[40:43], v[48:51], v[132:135]\n"
      "v_mfma_f32_16x16x32_bf16 v[124:127], a[44:47], v[48:51], v[124:127]\n"
      "ds_read_b128 v[36:39], v11 offset:57344\n"
      "v_mfma_f32_16x16x32_bf16 v[124:127], a[60:63], v[52:55], v[124:127]\n"
      "buffer_load_dwordx4 v2, s[40:43], s62 offen offset:1024 lds\n"
      "v_mfma_f32_16x16x32_bf16 v[120:123], a[52:55], v[52:55], v[120:123]\n"
      "s_nop 3\n"
      "v_mfma_f32_16x16x32_bf16 v[120:123], a[36:39], v[48:51], v[120:123]\n"
      "s_waitcnt lgkmcnt(3)\n"
      "v_mfma_f32_16x16x32_bf16 v[152:155], a[32:35], v[56:59], v[152:155]\n"
      "ds_read_b128 v[40:43], v10 offset:59392\n"
      "s_waitcnt lgkmcnt(3)\n"
      "v_mfma_f32_16x16x32_bf16 v[152:155], a[48:51], v[60:63], v[152:155]\n"
      "buffer_load_dwordx4 v2, s[40:43], s62 offen offset:2048 lds\n"
      "v_mfma_f32_16x16x32_bf16 v[156:159], a[56:59], v[60:63], v[156:159]\n"
      "s_nop 3\n"
      "v_mfma_f32_16x16x32_bf16 v[156:159], a[40:43], v[56:59], v[156:159]\n"
      "v_mfma_f32_16x16x32_bf16 v[148:151], a[44:47], v[56:59], v[148:151]\n"
      "ds_read_b128 v[44:47], v11 offset:59392\n"
      "v_mfma_f32_16x16x32_bf16 v[148:151], a[60:63], v[60:63], v[148:151]\n"
      "v_mfma_f32_16x16x32_bf16 v[144:147], a[52:55], v[60:63], v[144:147]\n"
      "s_nop 3\n"
      "v_mfma_f32_16x16x32_bf16 v[144:147], a[36:39], v[56:59], v[144:147]\n"
      "s_waitcnt lgkmcnt(3)\n"
      "v_mfma_f32_16x16x32_bf16 v[176:179], a[32:35], v[32:35], v[176:179]\n"
      "ds_read_b128 v[48:51], v10 offset:61440\n"
      "s_waitcnt lgkmcnt(3)\n"
      "v_mfma_f32_16x16x32_bf16 v[176:179], a[48:51], v[36:39], v[176:179]\n"
      "buffer_load_dwordx4 v2, s[40:43], s62 offen offset:3072 lds\n"
      "v_mfma_f32_16x16x32_bf16 v[180:183], a[56:59], v[36:39], v[180:183]\n"
      "s_nop 3\n"
      "v_mfma_f32_16x16x32_bf16 v[180:183], a[40:43], v[32:35], v[180:183]\n"
      "v_mfma_f32_16x16x32_bf16 v[172:175], a[44:47], v[32:35], v[172:175]\n"
      "ds_read_b128 v[52:55], v11 offset:61440\n"
      "v_mfma_f32_16x16x32_bf16 v[172:175], a[60:63], v[36:39], v[172:175]\n"
      "s_add_u32 m0, s59, 73728\n"
      "s_add_u32 s62, s57, 73728\n"
      "buffer_load_dwordx4 v2, s[40:43], s62 offen offset:0 lds\n"
      "v_mfma_f32_16x16x32_bf16 v[168:171], a[52:55], v[36:39], v[168:171]\n"
      "s_nop 3\n"
      "v_mfma_f32_16x16x32_bf16 v[168:171], a[36:39], v[32:35], v[168:171]\n"
      "s_waitcnt lgkmcnt(3)\n"
      "v_mfma_f32_16x16x32_bf16 v[200:203], a[32:35], v[40:43], v[200:203]\n"
      "ds_read_b128 v[56:59], v10 offset:63488\n"
      "s_waitcnt lgkmcnt(3)\n"
      "v_mfma_f32_16x16x32_bf16 v[200:203], a[48:51], v[44:47], v[200:203]\n"
      "v_mfma_f32_16x16x32_bf16 v[204:207], a[56:59], v[44:47], v[204:207]\n"
      "s_nop 3\n"
      "v_mfma_f32_16x16x32_bf16 v[204:207], a[40:43], v[40:43], v[204:207]\n"
      "v_mfma_f32_16x16x32_bf16 v[196:199], a[44:47], v[40:43], v[196:199]\n"
      "ds_read_b128 v[60:63], v11 offset:63488\n"
      "v_mfma_f32_16x16x32_bf16 v[196:199], a[60:63], v[44:47], v[196:199]\n"
      "buffer_load_dwordx4 v2, s[40:43], s62 offen offset:1024 lds\n"
      "v_mfma_f32_16x16x32_bf16 v[192:195], a[52:55], v[44:47], v[192:195]\n"
      "s_nop 3\n"
      "v_mfma_f32_16x16x32_bf16 v[192:195], a[36:39], v[40:43], v[192:195]\n"
      "s_waitcnt lgkmcnt(3)\n"
      "v_mfma_f32_16x16x32_bf16 v[224:227], a[32:35], v[48:51], v[224:227]\n"
      "s_waitcnt vmcnt(22)\n"
      "s_barrier\n"
      "ds_read_b128 v[32:35], v12 offset:0\n"
      "s_waitcnt lgkmcnt(3)\n"
      "v_mfma_f32_16x16x32_bf16 v[224:227], a[48:51], v[52:55], v[224:227]\n"
      "buffer_load_dwordx4 v2, s[40:43], s62 offen offset:2048 lds\n"
      "v_mfma_f32_16x16x32_bf16 v[228:231], a[56:59], v[52:55], v[228:231]\n"
      "s_nop 3\n"
      "v_mfma_f32_16x16x32_bf16 v[228:231], a[40:43], v[48:51], v[228:231]\n"
      "v_mfma_f32_16x16x32_bf16 v[220:223], a[44:47], v[48:51], v[220:223]\n"
      "ds_read_b128 v[36:39], v13 offset:0\n"
      "v_mfma_f32_16x16x32_bf16 v[220:223], a[60:63], v[52:55], v[220:223]\n"
      "v_mfma_f32_16x16x32_bf16 v[216:219], a[52:55], v[52:55], v[216:219]\n"
      "s_nop 3\n"
      "v_mfma_f32_16x16x32_bf16 v[216:219], a[36:39], v[48:51], v[216:219]\n"
      "s_waitcnt lgkmcnt(3)\n"
      "v_mfma_f32_16x16x32_bf16 v[248:251], a[32:35], v[56:59], v[248:251]\n"
      "ds_read_b128 v[40:43], v12 offset:16384\n"
      "s_waitcnt lgkmcnt(3)\n"
      "v_mfma_f32_16x16x32_bf16 v[248:251], a[48:51], v[60:63], v[248:251]\n"
      "buffer_load_dwordx4 v2, s[40:43], s62 offen offset:3072 lds\n"
      "v_mfma_f32_16x16x32_bf16 v[252:255], a[56:59], v[60:63], v[252:255]\n"
      "s_nop 3\n"
      "v_mfma_f32_16x16x32_bf16 v[252:255], a[40:43], v[56:59], v[252:255]\n"
      "v_mfma_f32_16x16x32_bf16 v[244:247], a[44:47], v[56:59], v[244:247]\n"
      "ds_read_b128 v[44:47], v13 offset:16384\n"
      "v_mfma_f32_16x16x32_bf16 v[244:247], a[60:63], v[60:63], v[244:247]\n"
      "s_add_u32 m0, s59, 77824\n"
      "s_add_u32 s62, s57, 77824\n"
      "buffer_load_dwordx4 v2, s[40:43], s62 offen offset:0 lds\n"
      "v_mfma_f32_16x16x32_bf16 v[240:243], a[52:55], v[60:63], v[240:243]\n"
      "s_nop 3\n"
      "v_mfma_f32_16x16x32_bf16 v[240:243], a[36:39], v[56:59], v[240:243]\n"
      "s_waitcnt vmcnt(21)\n"
      "s_waitcnt lgkmcnt(3)\n"
      "v_mfma_f32_16x16x32_bf16 v[64:67], a[64:67], v[32:35], v[64:67]\n"
      "ds_read_b128 v[48:51], v12 offset:32768\n"
      "s_waitcnt vmcnt(17)\n"
      "s_waitcnt lgkmcnt(3)\n"
      "v_mfma_f32_16x16x32_bf16 v[64:67], a[80:83], v[36:39], v[64:67]\n"
      "v_mfma_f32_16x16x32_bf16 v[68:71], a[88:91], v[36:39], v[68:71]\n"
      "s_nop 3\n"
      "v_mfma_f32_16x16x32_bf16 v[68:71], a[72:75], v[32:35], v[68:71]\n"
      "v_mfma_f32_16x16x32_bf16 v[84:87], a[76:79], v[32:35], v[84:87]\n"
      "ds_read_b128 v[52:55], v13 offset:32768\n"
      "v_mfma_f32_16x16x32_bf16 v[84:87], a[92:95], v[36:39], v[84:87]\n"
      "buffer_load_dwordx4 v2, s[40:43], s62 offen offset:1024 lds\n"
      "v_mfma_f32_16x16x32_bf16 v[80:83], a[84:87], v[36:39], v[80:83]\n"
      "s_nop 3\n"
      "v_mfma_f32_16x16x32_bf16 v[80:83], a[68:71], v[32:35], v[80:83]\n"
      "s_waitcnt lgkmcnt(3)\n"
      "v_mfma_f32_16x16x32_bf16 v[80:83], a[64:67], v[40:43], v[80:83]\n"
      "ds_read_b128 v[56:59], v12 offset:49152\n"
      "s_waitcnt lgkmcnt(3)\n"
      "v_mfma_f32_16x16x32_bf16 v[80:83], a[80:83], v[44:47], v[80:83]\n"
      "buffer_load_dwordx4 v2, s[40:43], s62 offen offset:2048 lds\n"
      "v_mfma_f32_16x16x32_bf16 v[84:87], a[88:91], v[44:47], v[84:87]\n"
      "s_nop 3\n"
      "v_mfma_f32_16x16x32_bf16 v[84:87], a[72:75], v[40:43], v[84:87]\n"
      "v_mfma_f32_16x16x32_bf16 v[76:79], a[76:79], v[40:43], v[76:79]\n"
      "ds_read_b128 v[60:63], v13 offset:49152\n"
      "v_mfma_f32_16x16x32_bf16 v[76:79], a[92:95], v[44:47], v[76:79]\n"
      "v_mfma_f32_16x16x32_bf16 v[72:75], a[84:87], v[44:47], v[72:75]\n"
      "s_nop 3\n"
      "v_mfma_f32_16x16x32_bf16 v[72:75], a[68:71], v[40:43], v[72:75]\n"
      "s_waitcnt vmcnt(15)\n"
      "s_waitcnt lgkmcnt(3)\n"
      "v_mfma_f32_16x16x32_bf16 v[64:67], a[96:99], v[48:51], v[64:67]\n"
      "ds_read_b128 v[32:35], v12 offset:2048\n"
      "s_waitcnt vmcnt(11)\n"
      "s_waitcnt lgkmcnt(3)\n"
      "v_mfma_f32_16x16x32_bf16 v[64:67], a[112:115], v[52:55], v[64:67]\n"
      "buffer_load_dwordx4 v4, s[40:43], s62 offen offset:3072 lds\n"
      "v_mfma_f32_16x16x32_bf16 v[68:71], a[120:123], v[52:55], v[68:71]\n"
      "s_mov_b32 m0, s75\n"
      "s_lshl_b32 s62, s52, 11\n"
      "buffer_load_dwordx4 v2, s[48:51], s62 offen offset:0 lds\n"
      "v_mfma_f32_16x16x32_bf16 v[68:71], a[104:107], v[48:51], v[68:71]\n"
      "v_mfma_f32_16x16x32_bf16 v[84:87], a[108:111], v[48:51], v[84:87]\n"
      "ds_read_b128 v[36:39], v13 offset:2048\n"
      "v_mfma_f32_16x16x32_bf16 v[84:87], a[124:127], v[52:55], v[84:87]\n"
      "buffer_load_dwordx4 v2, s[48:51], s62 offen offset:1024 lds\n"
      "v_mfma_f32_16x16x32_bf16 v[80:83], a[116:119], v[52:55], v[80:83]\n"
      "s_nop 3\n"
      "v_mfma_f32_16x16x32_bf16 v[80:83], a[100:103], v[48:51], v[80:83]\n"
      "s_waitcnt lgkmcnt(3)\n"
      "v_mfma_f32_16x16x32_bf16 v[80:83], a[96:99], v[56:59], v[80:83]\n"
      "ds_read_b128 v[40:43], v12 offset:34816\n"
      "s_waitcnt lgkmcnt(3)\n"
      "v_mfma_f32_16x16x32_bf16 v[80:83], a[112:115], v[60:63], v[80:83]\n"
      "v_mfma_f32_16x16x32_bf16 v[84:87], a[120:123], v[60:63], v[84:87]\n"
      "s_nop 3\n"
      "v_mfma_f32_16x16x32_bf16 v[84:87], a[104:107], v[56:59], v[84:87]\n"
      "v_mfma_f32_16x16x32_bf16 v[76:79], a[108:111], v[56:59], v[76:79]\n"
      "ds_read_b128 v[44:47], v13 offset:34816\n"
      "v_mfma_f32_16x16x32_bf16 v[76:79], a[124:127], v[60:63], v[76:79]\n"
      "v_mfma_f32_16x16x32_bf16 v[72:75], a[116:119], v[60:63], v[72:75]\n"
      "s_nop 3\n"
      "v_mfma_f32_16x16x32_bf16 v[72:75], a[100:103], v[56:59], v[72:75]\n"
      "s_waitcnt lgkmcnt(3)\n"
      "v_mfma_f32_16x16x32_bf16 v[88:91], a[64:67], v[32:35], v[88:91]\n"
      "ds_read_b128 v[48:51], v12 offset:18432\n"
      "s_waitcnt lgkmcnt(3)\n"
      "v_mfma_f32_16x16x32_bf16 v[88:91], a[80:83], v[36:39], v[88:91]\n"
      "s_waitcnt lgkmcnt(2)\n"
      "v_mfma_f32_16x16x32_bf16 v[88:91], a[96:99], v[40:43], v[88:91]\n"
      "s_nop 3\n"
      "s_waitcnt lgkmcnt(1)\n"
      "v_mfma_f32_16x16x32_bf16 v[88:91], a[112:115], v[44:47], v[88:91]\n"
      "v_sub_f32 v64, v64, v72\n"
      "v_mfma_f32_16x16x32_bf16 v[92:95], a[120:123], v[44:47], v[92:95]\n"
      "ds_read_b128 v[52:55], v13 offset:18432\n"
      "v_sub_f32 v65, v65, v73\n"
      "v_mfma_f32_16x16x32_bf16 v[92:95], a[104:107], v[40:43], v[92:95]\n"
      "v_sub_f32 v66, v66, v74\n"
      "v_sub_f32 v67, v67, v75\n"
      "v_mfma_f32_16x16x32_bf16 v[92:95], a[88:91], v[36:39], v[92:95]\n"
      "v_cvt_pk_bf16_f32 v24, v64, v80\n"
      "v_cvt_pk_bf16_f32 v25, v65, v81\n"
      "v_mfma_f32_16x16x32_bf16 v[92:95], a[72:75], v[32:35], v[92:95]\n"
      "v_cvt_pk_bf16_f32 v26, v66, v82\n"
      "v_mfma_f32_16x16x32_bf16 v[108:111], a[76:79], v[32:35], v[108:111]\n"
      "ds_read_b128 v[56:59], v12 offset:51200\n"
      "v_cvt_pk_bf16_f32 v27, v67, v83\n"
      "v_mfma_f32_16x16x32_bf16 v[108:111], a[92:95], v[36:39], v[108:111]\n"
      "s_add_u32 s64, s61, 0\n"
      "buffer_store_dwordx4 v[24:27], v5, s[40:43], s64 offen offset:0 sc1\n"
      "v_mfma_f32_16x16x32_bf16 v[108:111], a[108:111], v[40:43], v[108:111]\n"
      "v_sub_f32 v68, v68, v76\n"
      "v_sub_f32 v69, v69, v77\n"
      "v_mfma_f32_16x16x32_bf16 v[108:111], a[124:127], v[44:47], v[108:111]\n"
      "v_sub_f32 v70, v70, v78\n"
      "v_mfma_f32_16x16x32_bf16 v[104:107], a[116:119], v[44:47], v[104:107]\n"
      "ds_read_b128 v[60:63], v13 offset:51200\n"
      "v_sub_f32 v71, v71, v79\n"
      "v_mfma_f32_16x16x32_bf16 v[104:107], a[100:103], v[40:43], v[104:107]\n"
      "v_cvt_pk_bf16_f32 v28, v68, v84\n"
      "v_cvt_pk_bf16_f32 v29, v69, v85\n"
      "v_mfma_f32_16x16x32_bf16 v[104:107], a[84:87], v[36:39], v[104:107]\n"
      "v_cvt_pk_bf16_f32 v30, v70, v86\n"
      "v_cvt_pk_bf16_f32 v31, v71, v87\n"
      "v_mfma_f32_16x16x32_bf16 v[104:107], a[68:71], v[32:35], v[104:107]\n"
      "s_waitcnt lgkmcnt(3)\n"
      "v_mfma_f32_16x16x32_bf16 v[104:107], a[64:67], v[48:51], v[104:107]\n"
      "s_waitcnt vmcnt(11)\n"
      "s_barrier\n"
      "ds_read_b128 v[32:35], v12 offset:4096\n"
      "s_waitcnt lgkmcnt(3)\n"
      "v_mfma_f32_16x16x32_bf16 v[104:107], a[80:83], v[52:55], v[104:107]\n"
      "buffer_store_dwordx4 v[28:31], v5, s[40:43], s64 offen offset:64 sc1\n"
      "s_waitcnt lgkmcnt(2)\n"
      "v_mfma_f32_16x16x32_bf16 v[104:107], a[96:99], v[56:59], v[104:107]\n"
      "s_nop 3\n"
      "s_waitcnt lgkmcnt(1)\n"
      "v_mfma_f32_16x16x32_bf16 v[104:107], a[112:115], v[60:63], v[104:107]\n"
      "v_mfma_f32_16x16x32_bf16 v[108:111], a[120:123], v[60:63], v[108:111]\n"
      "ds_read_b128 v[36:39], v13 offset:4096\n"
      "v_mfma_f32_16x16x32_bf16 v[108:111], a[104:107], v[56:59], v[108:111]\n"
      "v_mfma_f32_16x16x32_bf16 v[108:111], a[88:91], v[52:55], v[108:111]\n"
      "s_nop 3\n"
      "v_mfma_f32_16x16x32_bf16 v[108:111], a[72:75], v[48:51], v[108:111]\n"
      "v_mfma_f32_16x16x32_bf16 v[100:103], a[76:79], v[48:51], v[100:103]\n"
      "ds_read_b128 v[40:43], v12 offset:36864\n"
      "v_mfma_f32_16x16x32_bf16 v[100:103], a[92:95], v[52:55], v[100:103]\n"
      "v_mfma_f32_16x16x32_bf16 v[100:103], a[108:111], v[56:59], v[100:103]\n"
      "s_nop 3\n"
      "v_mfma_f32_16x16x32_bf16 v[100:103], a[124:127], v[60:63], v[100:103]\n"
      "v_mfma_f32_16x16x32_bf16 v[96:99], a[116:119], v[60:63], v[96:99]\n"
      "ds_read_b128 v[44:47], v13 offset:36864\n"
      "v_mfma_f32_16x16x32_bf16 v[96:99], a[100:103], v[56:59], v[96:99]\n"
      "v_mfma_f32_16x16x32_bf16 v[96:99], a[84:87], v[52:55], v[96:99]\n"
      "s_nop 3\n"
      "v_mfma_f32_16x16x32_bf16 v[96:99], a[68:71], v[48:51], v[96:99]\n"
      "s_waitcnt lgkmcnt(3)\n"
      "v_mfma_f32_16x16x32_bf16 v[112:115], a[64:67], v[32:35], v[112:115]\n"
      "ds_read_b128 v[48:51], v12 offset:20480\n"
      "s_waitcnt lgkmcnt(3)\n"
      "v_mfma_f32_16x16x32_bf16 v[112:115], a[80:83], v[36:39], v[112:115]\n"
      "s_waitcnt lgkmcnt(2)\n"
      "v_mfma_f32_16x16x32_bf16 v[112:115], a[96:99], v[40:43], v[112:115]\n"
      "s_nop 3\n"
      "s_waitcnt lgkmcnt(1)\n"
      "v_mfma_f32_16x16x32_bf16 v[112:115], a[112:115], v[44:47], v[112:115]\n"
      "v_sub_f32 v88, v88, v96\n"
      "v_mfma_f32_16x16x32_bf16 v[116:119], a[120:123], v[44:47], v[116:119]\n"
      "ds_read_b128 v[52:55], v13 offset:20480\n"
      "v_sub_f32 v89, v89, v97\n"
      "v_mfma_f32_16x16x32_bf16 v[116:119], a[104:107], v[40:43], v[116:119]\n"
      "v_sub_f32 v90, v90, v98\n"
      "v_sub_f32 v91, v91, v99\n"
      "v_mfma_f32_16x16x32_bf16 v[116:119], a[88:91], v[36:39], v[116:119]\n"
      "v_cvt_pk_bf16_f32 v24, v88, v104\n"
      "v_cvt_pk_bf16_f32 v25, v89, v105\n"
      "v_mfma_f32_16x16x32_bf16 v[116:119], a[72:75], v[32:35], v[116:119]\n"
      "v_cvt_pk_bf16_f32 v26, v90, v106\n"
      "v_mfma_f32_16x16x32_bf16 v[132:135], a[76:79], v[32:35], v[132:135]\n"
      "ds_read_b128 v[56:59], v12 offset:53248\n"
      "v_cvt_pk_bf16_f32 v27, v91, v107\n"
      "v_mfma_f32_16x16x32_bf16 v[132:135], a[92:95], v[36:39], v[132:135]\n"
      "s_add_u32 s64, s61, 1015808\n"
      "buffer_store_dwordx4 v[24:27], v5, s[40:43], s64 offen offset:0 sc1\n"
      "v_mfma_f32_16x16x32_bf16 v[132:135], a[108:111], v[40:43], v[132:135]\n"
      "v_sub_f32 v92, v92, v100\n"
      "v_sub_f32 v93, v93, v101\n"
      "v_mfma_f32_16x16x32_bf16 v[132:135], a[124:127], v[44:47], v[132:135]\n"
      "v_sub_f32 v94, v94, v102\n"
      "v_mfma_f32_16x16x32_bf16 v[128:131], a[116:119], v[44:47], v[128:131]\n"
      "ds_read_b128 v[60:63], v13 offset:53248\n"
      "v_sub_f32 v95, v95, v103\n"
      "v_mfma_f32_16x16x32_bf16 v[128:131], a[100:103], v[40:43], v[128:131]\n"
      "v_cvt_pk_bf16_f32 v28, v92, v108\n"
      "v_cvt_pk_bf16_f32 v29, v93, v109\n"
      "v_mfma_f32_16x16x32_bf16 v[128:131], a[84:87], v[36:39], v[128:131]\n"
      "v_cvt_pk_bf16_f32 v30, v94, v110\n"
      "v_cvt_pk_bf16_f32 v31, v95, v111\n"
      "v_mfma_f32_16x16x32_bf16 v[128:131], a[68:71], v[32:35], v[128:131]\n"
      "s_waitcnt lgkmcnt(3)\n"
      "v_mfma_f32_16x16x32_bf16 v[128:131], a[64:67], v[48:51], v[128:131]\n"
      "ds_read_b128 v[32:35], v12 offset:6144\n"
      "s_waitcnt lgkmcnt(3)\n"
      "v_mfma_f32_16x16x32_bf16 v[128:131], a[80:83], v[52:55], v[128:131]\n"
      "buffer_store_dwordx4 v[28:31], v5, s[40:43], s64 offen offset:64 sc1\n"
      "s_waitcnt lgkmcnt(2)\n"
      "v_mfma_f32_16x16x32_bf16 v[128:131], a[96:99], v[56:59], v[128:131]\n"
      "s_nop 3\n"
      "s_waitcnt lgkmcnt(1)\n"
      "v_mfma_f32_16x16x32_bf16 v[128:131], a[112:115], v[60:63], v[128:131]\n"
      "v_mfma_f32_16x16x32_bf16 v[132:135], a[120:123], v[60:63], v[132:135]\n"
      "ds_read_b128 v[36:39], v13 offset:6144\n"
      "v_mfma_f32_16x16x32_bf16 v[132:135], a[104:107], v[56:59], v[132:135]\n"
      "v_mfma_f32_16x16x32_bf16 v[132:135], a[88:91], v[52:55], v[132:135]\n"
      "s_nop 3\n"
      "v_mfma_f32_16x16x32_bf16 v[132:135], a[72:75], v[48:51], v[132:135]\n"
      "v_mfma_f32_16x16x32_bf16 v[124:127], a[76:79], v[48:51], v[124:127]\n"
      "ds_read_b128 v[40:43], v12 offset:38912\n"
      "v_mfma_f32_16x16x32_bf16 v[124:127], a[92:95], v[52:55], v[124:127]\n"
      "v_mfma_f32_16x16x32_bf16 v[124:127], a[108:111], v[56:59], v[124:127]\n"
      "s_nop 3\n"
      "v_mfma_f32_16x16x32_bf16 v[124:127], a[124:127], v[60:63], v[124:127]\n"
      "v_mfma_f32_16x16x32_bf16 v[120:123], a[116:119], v[60:63], v[120:123]\n"
      "ds_read_b128 v[44:47], v13 offset:38912\n"
      "v_mfma_f32_16x16x32_bf16 v[120:123], a[100:103], v[56:59], v[120:123]\n"
      "v_mfma_f32_16x16x32_bf16 v[120:123], a[84:87], v[52:55], v[120:123]\n"
      "s_nop 3\n"
      "v_mfma_f32_16x16x32_bf16 v[120:123], a[68:71], v[48:51], v[120:123]\n"
      "s_waitcnt lgkmcnt(3)\n"
      "v_mfma_f32_16x16x32_bf16 v[136:139], a[64:67], v[32:35], v[136:139]\n"
      "ds_read_b128 v[48:51], v12 offset:22528\n"
      "s_waitcnt lgkmcnt(3)\n"
      "v_mfma_f32_16x16x32_bf16 v[136:139], a[80:83], v[36:39], v[136:139]\n"
      "s_waitcnt lgkmcnt(2)\n"
      "v_mfma_f32_16x16x32_bf16 v[136:139], a[96:99], v[40:43], v[136:139]\n"
      "s_nop 3\n"
      "s_waitcnt lgkmcnt(1)\n"
      "v_mfma_f32_16x16x32_bf16 v[136:139], a[112:115], v[44:47], v[136:139]\n"
      "v_sub_f32 v112, v112, v120\n"
      "v_mfma_f32_16x16x32_bf16 v[140:143], a[120:123], v[44:47], v[140:143]\n"
      "ds_read_b128 v[52:55], v13 offset:22528\n"
      "v_sub_f32 v113, v113, v121\n"
      "v_mfma_f32_16x16x32_bf16 v[140:143], a[104:107], v[40:43], v[140:143]\n"
      "v_sub_f32 v114, v114, v122\n"
      "v_sub_f32 v115, v115, v123\n"
      "v_mfma_f32_16x16x32_bf16 v[140:143], a[88:91], v[36:39], v[140:143]\n"
      "v_cvt_pk_bf16_f32 v24, v112, v128\n"
      "v_cvt_pk_bf16_f32 v25, v113, v129\n"
      "v_mfma_f32_16x16x32_bf16 v[140:143], a[72:75], v[32:35], v[140:143]\n"
      "v_cvt_pk_bf16_f32 v26, v114, v130\n"
      "v_mfma_f32_16x16x32_bf16 v[156:159], a[76:79], v[32:35], v[156:159]\n"
      "ds_read_b128 v[56:59], v12 offset:55296\n"
      "v_cvt_pk_bf16_f32 v27, v115, v131\n"
      "v_mfma_f32_16x16x32_bf16 v[156:159], a[92:95], v[36:39], v[156:159]\n"
      "s_add_u32 s64, s61, 2031616\n"
      "buffer_store_dwordx4 v[24:27], v5, s[40:43], s64 offen offset:0 sc1\n"
      "v_mfma_f32_16x16x32_bf16 v[156:159], a[108:111], v[40:43], v[156:159]\n"
      "v_sub_f32 v116, v116, v124\n"
      "v_sub_f32 v117, v117, v125\n"
      "v_mfma_f32_16x16x32_bf16 v[156:159], a[124:127], v[44:47], v[156:159]\n"
      "v_sub_f32 v118, v118, v126\n"
      "v_mfma_f32_16x16x32_bf16 v[152:155], a[116:119], v[44:47], v[152:155]\n"
      "ds_read_b128 v[60:63], v13 offset:55296\n"
      "v_sub_f32 v119, v119, v127\n"
      "v_mfma_f32_16x16x32_bf16 v[152:155], a[100:103], v[40:43], v[152:155]\n"
      "v_cvt_pk_bf16_f32 v28, v116, v132\n"
      "v_cvt_pk_bf16_f32 v29, v117, v133\n"
      "v_mfma_f32_16x16x32_bf16 v[152:155], a[84:87], v[36:39], v[152:155]\n"
      "v_cvt_pk_bf16_f32 v30, v118, v134\n"
      "v_cvt_pk_bf16_f32 v31, v119, v135\n"
      "v_mfma_f32_16x16x32_bf16 v[152:155], a[68:71], v[32:35], v[152:155]\n"
      "s_waitcnt lgkmcnt(3)\n"
      "v_mfma_f32_16x16x32_bf16 v[152:155], a[64:67], v[48:51], v[152:155]\n"
      "s_waitcnt vmcnt(11)\n"
      "s_barrier\n"
      "ds_read_b128 v[32:35], v12 offset:8192\n"
      "s_waitcnt lgkmcnt(3)\n"
      "v_mfma_f32_16x16x32_bf16 v[152:155], a[80:83], v[52:55], v[152:155]\n"
      "buffer_store_dwordx4 v[28:31], v5, s[40:43], s64 offen offset:64 sc1\n"
      "s_waitcnt lgkmcnt(2)\n"
      "v_mfma_f32_16x16x32_bf16 v[152:155], a[96:99], v[56:59], v[152:155]\n"
      "s_nop 3\n"
      "s_waitcnt lgkmcnt(1)\n"
      "v_mfma_f32_16x16x32_bf16 v[152:155], a[112:115], v[60:63], v[152:155]\n"
      "v_mfma_f32_16x16x32_bf16 v[156:159], a[120:123], v[60:63], v[156:159]\n"
      "ds_read_b128 v[36:39], v13 offset:8192\n"
      "v_mfma_f32_16x16x32_bf16 v[156:159], a[104:107], v[56:59], v[156:159]\n"
      "v_mfma_f32_16x16x32_bf16 v[156:159], a[88:91], v[52:55], v[156:159]\n"
      "s_nop 3\n"
      "v_mfma_f32_16x16x32_bf16 v[156:159], a[72:75], v[48:51], v[156:159]\n"
      "v_mfma_f32_16x16x32_bf16 v[148:151], a[76:79], v[48:51], v[148:151]\n"
      "ds_read_b128 v[40:43], v12 offset:40960\n"
      "v_mfma_f32_16x16x32_bf16 v[148:151], a[92:95], v[52:55], v[148:151]\n"
      "v_mfma_f32_16x16x32_bf16 v[148:151], a[108:111], v[56:59], v[148:151]\n"
      "s_nop 3\n"
      "v_mfma_f32_16x16x32_bf16 v[148:151], a[124:127], v[60:63], v[148:151]\n"
      "v_mfma_f32_16x16x32_bf16 v[144:147], a[116:119], v[60:63], v[144:147]\n"
      "ds_read_b128 v[44:47], v13 offset:40960\n"
      "v_mfma_f32_16x16x32_bf16 v[144:147], a[100:103], v[56:59], v[144:147]\n"
      "v_mfma_f32_16x16x32_bf16 v[144:147], a[84:87], v[52:55], v[144:147]\n"
      "s_nop 3\n"
      "v_mfma_f32_16x16x32_bf16 v[144:147], a[68:71], v[48:51], v[144:147]\n"
      "s_waitcnt lgkmcnt(3)\n"
      "v_mfma_f32_16x16x32_bf16 v[160:163], a[64:67], v[32:35], v[160:163]\n"
      "ds_read_b128 v[48:51], v12 offset:24576\n"
      "s_waitcnt lgkmcnt(3)\n"
      "v_mfma_f32_16x16x32_bf16 v[160:163], a[80:83], v[36:39], v[160:163]\n"
      "s_waitcnt lgkmcnt(2)\n"
      "v_mfma_f32_16x16x32_bf16 v[160:163], a[96:99], v[40:43], v[160:163]\n"
      "s_nop 3\n"
      "s_waitcnt lgkmcnt(1)\n"
      "v_mfma_f32_16x16x32_bf16 v[160:163], a[112:115], v[44:47], v[160:163]\n"
      "v_sub_f32 v136, v136, v144\n"
      "v_mfma_f32_16x16x32_bf16 v[164:167], a[120:123], v[44:47], v[164:167]\n"
      "ds_read_b128 v[52:55], v13 offset:24576\n"
      "v_sub_f32 v137, v137, v145\n"
      "v_mfma_f32_16x16x32_bf16 v[164:167], a[104:107], v[40:43], v[164:167]\n"
      "v_sub_f32 v138, v138, v146\n"
      "v_sub_f32 v139, v139, v147\n"
      "v_mfma_f32_16x16x32_bf16 v[164:167], a[88:91], v[36:39], v[164:167]\n"
      "v_cvt_pk_bf16_f32 v24, v136, v152\n"
      "v_cvt_pk_bf16_f32 v25, v137, v153\n"
      "v_mfma_f32_16x16x32_bf16 v[164:167], a[72:75], v[32:35], v[164:167]\n"
      "v_cvt_pk_bf16_f32 v26, v138, v154\n"
      "v_mfma_f32_16x16x32_bf16 v[180:183], a[76:79], v[32:35], v[180:183]\n"
      "ds_read_b128 v[56:59], v12 offset:57344\n"
      "v_cvt_pk_bf16_f32 v27, v139, v155\n"
      "v_mfma_f32_16x16x32_bf16 v[180:183], a[92:95], v[36:39], v[180:183]\n"
      "s_add_u32 s64, s61, 3047424\n"
      "buffer_store_dwordx4 v[24:27], v5, s[40:43], s64 offen offset:0 sc1\n"
      "v_mfma_f32_16x16x32_bf16 v[180:183], a[108:111], v[40:43], v[180:183]\n"
      "v_sub_f32 v140, v140, v148\n"
      "v_sub_f32 v141, v141, v149\n"
      "v_mfma_f32_16x16x32_bf16 v[180:183], a[124:127], v[44:47], v[180:183]\n"
      "v_sub_f32 v142, v142, v150\n"
      "v_mfma_f32_16x16x32_bf16 v[176:179], a[116:119], v[44:47], v[176:179]\n"
      "ds_read_b128 v[60:63], v13 offset:57344\n"
      "v_sub_f32 v143, v143, v151\n"
      "v_mfma_f32_16x16x32_bf16 v[176:179], a[100:103], v[40:43], v[176:179]\n"
      "v_cvt_pk_bf16_f32 v28, v140, v156\n"
      "v_cvt_pk_bf16_f32 v29, v141, v157\n"
      "v_mfma_f32_16x16x32_bf16 v[176:179], a[84:87], v[36:39], v[176:179]\n"
      "v_cvt_pk_bf16_f32 v30, v142, v158\n"
      "v_cvt_pk_bf16_f32 v31, v143, v159\n"
      "v_mfma_f32_16x16x32_bf16 v[176:179], a[68:71], v[32:35], v[176:179]\n"
      "s_waitcnt lgkmcnt(3)\n"
      "v_mfma_f32_16x16x32_bf16 v[176:179], a[64:67], v[48:51], v[176:179]\n"
      "ds_read_b128 v[32:35], v12 offset:10240\n"
      "s_waitcnt lgkmcnt(3)\n"
      "v_mfma_f32_16x16x32_bf16 v[176:179], a[80:83], v[52:55], v[176:179]\n"
      "buffer_store_dwordx4 v[28:31], v5, s[40:43], s64 offen offset:64 sc1\n"
      "s_waitcnt lgkmcnt(2)\n"
      "v_mfma_f32_16x16x32_bf16 v[176:179], a[96:99], v[56:59], v[176:179]\n"
      "s_nop 3\n"
      "s_waitcnt lgkmcnt(1)\n"
      "v_mfma_f32_16x16x32_bf16 v[176:179], a[112:115], v[60:63], v[176:179]\n"
      "v_mfma_f32_16x16x32_bf16 v[180:183], a[120:123], v[60:63], v[180:183]\n"
      "ds_read_b128 v[36:39], v13 offset:10240\n"
      "v_mfma_f32_16x16x32_bf16 v[180:183], a[104:107], v[56:59], v[180:183]\n"
      "v_mfma_f32_16x16x32_bf16 v[180:183], a[88:91], v[52:55], v[180:183]\n"
      "s_nop 3\n"
      "v_mfma_f32_16x16x32_bf16 v[180:183], a[72:75], v[48:51], v[180:183]\n"
      "v_mfma_f32_16x16x32_bf16 v[172:175], a[76:79], v[48:51], v[172:175]\n"
      "ds_read_b128 v[40:43], v12 offset:43008\n"
      "v_mfma_f32_16x16x32_bf16 v[172:175], a[92:95], v[52:55], v[172:175]\n"
      "v_mfma_f32_16x16x32_bf16 v[172:175], a[108:111], v[56:59], v[172:175]\n"
      "s_nop 3\n"
      "v_mfma_f32_16x16x32_bf16 v[172:175], a[124:127], v[60:63], v[172:175]\n"
      "v_mfma_f32_16x16x32_bf16 v[168:171], a[116:119], v[60:63], v[168:171]\n"
      "ds_read_b128 v[44:47], v13 offset:43008\n"
      "v_mfma_f32_16x16x32_bf16 v[168:171], a[100:103], v[56:59], v[168:171]\n"
      "v_mfma_f32_16x16x32_bf16 v[168:171], a[84:87], v[52:55], v[168:171]\n"
      "s_nop 3\n"
      "v_mfma_f32_16x16x32_bf16 v[168:171], a[68:71], v[48:51], v[168:171]\n"
      "s_waitcnt lgkmcnt(3)\n"
      "v_mfma_f32_16x16x32_bf16 v[184:187], a[64:67], v[32:35], v[184:187]\n"
      "ds_read_b128 v[48:51], v12 offset:26624\n"
      "s_waitcnt lgkmcnt(3)\n"
      "v_mfma_f32_16x16x32_bf16 v[184:187], a[80:83], v[36:39], v[184:187]\n"
      "s_waitcnt lgkmcnt(2)\n"
      "v_mfma_f32_16x16x32_bf16 v[184:187], a[96:99], v[40:43], v[184:187]\n"
      "s_nop 3\n"
      "s_waitcnt lgkmcnt(1)\n"
      "v_mfma_f32_16x16x32_bf16 v[184:187], a[112:115], v[44:47], v[184:187]\n"
      "v_sub_f32 v160, v160, v168\n"
      "v_mfma_f32_16x16x32_bf16 v[188:191], a[120:123], v[44:47], v[188:191]\n"
      "ds_read_b128 v[52:55], v13 offset:26624\n"
      "v_sub_f32 v161, v161, v169\n"
      "v_mfma_f32_16x16x32_bf16 v[188:191], a[104:107], v[40:43], v[188:191]\n"
      "v_sub_f32 v162, v162, v170\n"
      "v_sub_f32 v163, v163, v171\n"
      "v_mfma_f32_16x16x32_bf16 v[188:191], a[88:91], v[36:39], v[188:191]\n"
      "v_cvt_pk_bf16_f32 v24, v160, v176\n"
      "v_cvt_pk_bf16_f32 v25, v161, v177\n"
      "v_mfma_f32_16x16x32_bf16 v[188:191], a[72:75], v[32:35], v[188:191]\n"
      "v_cvt_pk_bf16_f32 v26, v162, v178\n"
      "v_mfma_f32_16x16x32_bf16 v[204:207], a[76:79], v[32:35], v[204:207]\n"
      "ds_read_b128 v[56:59], v12 offset:59392\n"
      "v_cvt_pk_bf16_f32 v27, v163, v179\n"
      "v_mfma_f32_16x16x32_bf16 v[204:207], a[92:95], v[36:39], v[204:207]\n"
      "s_add_u32 s64, s61, 4063232\n"
      "buffer_store_dwordx4 v[24:27], v5, s[40:43], s64 offen offset:0 sc1\n"
      "v_mfma_f32_16x16x32_bf16 v[204:207], a[108:111], v[40:43], v[204:207]\n"
      "v_sub_f32 v164, v164, v172\n"
      "v_sub_f32 v165, v165, v173\n"
      "v_mfma_f32_16x16x32_bf16 v[204:207], a[124:127], v[44:47], v[204:207]\n"
      "v_sub_f32 v166, v166, v174\n"
      "v_mfma_f32_16x16x32_bf16 v[200:203], a[116:119], v[44:47], v[200:203]\n"
      "ds_read_b128 v[60:63], v13 offset:59392\n"
      "v_sub_f32 v167, v167, v175\n"
      "v_mfma_f32_16x16x32_bf16 v[200:203], a[100:103], v[40:43], v[200:203]\n"
      "v_cvt_pk_bf16_f32 v28, v164, v180\n"
      "v_cvt_pk_bf16_f32 v29, v165, v181\n"
      "v_mfma_f32_16x16x32_bf16 v[200:203], a[84:87], v[36:39], v[200:203]\n"
      "v_cvt_pk_bf16_f32 v30, v166, v182\n"
      "v_cvt_pk_bf16_f32 v31, v167, v183\n"
      "v_mfma_f32_16x16x32_bf16 v[200:203], a[68:71], v[32:35], v[200:203]\n"
      "s_waitcnt lgkmcnt(3)\n"
      "v_mfma_f32_16x16x32_bf16 v[200:203], a[64:67], v[48:51], v[200:203]\n"
      "s_waitcnt vmcnt(11)\n"
      "s_waitcnt vmcnt(5)\n"
      "s_barrier\n"
      "s_add_u32 s68, s66, 0\n"
      "buffer_store_dword v20, v1, s[40:43], s68 offen sc1\n"
      "ds_read_b128 v[32:35], v12 offset:12288\n"
      "s_waitcnt lgkmcnt(3)\n"
      "v_mfma_f32_16x16x32_bf16 v[200:203], a[80:83], v[52:55], v[200:203]\n"
      "buffer_store_dwordx4 v[28:31], v5, s[40:43], s64 offen offset:64 sc1\n"
      "s_waitcnt lgkmcnt(2)\n"
      "v_mfma_f32_16x16x32_bf16 v[200:203], a[96:99], v[56:59], v[200:203]\n"
      "s_nop 3\n"
      "s_waitcnt lgkmcnt(1)\n"
      "v_mfma_f32_16x16x32_bf16 v[200:203], a[112:115], v[60:63], v[200:203]\n"
      "v_mfma_f32_16x16x32_bf16 v[204:207], a[120:123], v[60:63], v[204:207]\n"
      "ds_read_b128 v[36:39], v13 offset:12288\n"
      "v_mfma_f32_16x16x32_bf16 v[204:207], a[104:107], v[56:59], v[204:207]\n"
      "v_mfma_f32_16x16x32_bf16 v[204:207], a[88:91], v[52:55], v[204:207]\n"
      "s_nop 3\n"
      "v_mfma_f32_16x16x32_bf16 v[204:207], a[72:75], v[48:51], v[204:207]\n"
      "v_mfma_f32_16x16x32_bf16 v[196:199], a[76:79], v[48:51], v[196:199]\n"
      "ds_read_b128 v[40:43], v12 offset:45056\n"
      "v_mfma_f32_16x16x32_bf16 v[196:199], a[92:95], v[52:55], v[196:199]\n"
      "v_mfma_f32_16x16x32_bf16 v[196:199], a[108:111], v[56:59], v[196:199]\n"
      "s_nop 3\n"
      "v_mfma_f32_16x16x32_bf16 v[196:199], a[124:127], v[60:63], v[196:199]\n"
      "v_mfma_f32_16x16x32_bf16 v[192:195], a[116:119], v[60:63], v[192:195]\n"
      "ds_read_b128 v[44:47], v13 offset:45056\n"
      "v_mfma_f32_16x16x32_bf16 v[192:195], a[100:103], v[56:59], v[192:195]\n"
      "v_mfma_f32_16x16x32_bf16 v[192:195], a[84:87], v[52:55], v[192:195]\n"
      "s_nop 3\n"
      "v_mfma_f32_16x16x32_bf16 v[192:195], a[68:71], v[48:51], v[192:195]\n"
      "s_waitcnt lgkmcnt(3)\n"
      "v_mfma_f32_16x16x32_bf16 v[208:211], a[64:67], v[32:35], v[208:211]\n"
      "ds_read_b128 v[48:51], v12 offset:28672\n"
      "s_waitcnt lgkmcnt(3)\n"
      "v_mfma_f32_16x16x32_bf16 v[208:211], a[80:83], v[36:39], v[208:211]\n"
      "s_waitcnt lgkmcnt(2)\n"
      "v_mfma_f32_16x16x32_bf16 v[208:211], a[96:99], v[40:43], v[208:211]\n"
      "s_nop 3\n"
      "s_waitcnt lgkmcnt(1)\n"
      "v_mfma_f32_16x16x32_bf16 v[208:211], a[112:115], v[44:47], v[208:211]\n"
      "v_sub_f32 v184, v184, v192\n"
      "v_mfma_f32_16x16x32_bf16 v[212:215], a[120:123], v[44:47], v[212:215]\n"
      "ds_read_b128 v[52:55], v13 offset:28672\n"
      "v_sub_f32 v185, v185, v193\n"
      "v_mfma_f32_16x16x32_bf16 v[212:215], a[104:107], v[40:43], v[212:215]\n"
      "v_sub_f32 v186, v186, v194\n"
      "v_sub_f32 v187, v187, v195\n"
      "v_mfma_f32_16x16x32_bf16 v[212:215], a[88:91], v[36:39], v[212:215]\n"
      "v_cvt_pk_bf16_f32 v24, v184, v200\n"
      "v_cvt_pk_bf16_f32 v25, v185, v201\n"
      "v_mfma_f32_16x16x32_bf16 v[212:215], a[72:75], v[32:35], v[212:215]\n"
      "v_cvt_pk_bf16_f32 v26, v186, v202\n"
      "v_mfma_f32_16x16x32_bf16 v[228:231], a[76:79], v[32:35], v[228:231]\n"
      "ds_read_b128 v[56:59], v12 offset:61440\n"
      "v_cvt_pk_bf16_f32 v27, v187, v203\n"
      "v_mfma_f32_16x16x32_bf16 v[228:231], a[92:95], v[36:39], v[228:231]\n"
      "s_add_u32 s64, s61, 5079040\n"
      "buffer_store_dwordx4 v[24:27], v5, s[40:43], s64 offen offset:0 sc1\n"
      "v_mfma_f32_16x16x32_bf16 v[228:231], a[108:111], v[40:43], v[228:231]\n"
      "v_sub_f32 v188, v188, v196\n"
      "v_sub_f32 v189, v189, v197\n"
      "v_mfma_f32_16x16x32_bf16 v[228:231], a[124:127], v[44:47], v[228:231]\n"
      "v_sub_f32 v190, v190, v198\n"
      "v_mfma_f32_16x16x32_bf16 v[224:227], a[116:119], v[44:47], v[224:227]\n"
      "ds_read_b128 v[60:63], v13 offset:61440\n"
      "v_sub_f32 v191, v191, v199\n"
      "v_mfma_f32_16x16x32_bf16 v[224:227], a[100:103], v[40:43], v[224:227]\n"
      "v_cvt_pk_bf16_f32 v28, v188, v204\n"
      "v_cvt_pk_bf16_f32 v29, v189, v205\n"
      "v_mfma_f32_16x16x32_bf16 v[224:227], a[84:87], v[36:39], v[224:227]\n"
      "v_cvt_pk_bf16_f32 v30, v190, v206\n"
      "v_cvt_pk_bf16_f32 v31, v191, v207\n"
      "v_mfma_f32_16x16x32_bf16 v[224:227], a[68:71], v[32:35], v[224:227]\n"
      "s_waitcnt lgkmcnt(3)\n"
      "v_mfma_f32_16x16x32_bf16 v[224:227], a[64:67], v[48:51], v[224:227]\n"
      "ds_read_b128 v[32:35], v12 offset:14336\n"
      "s_waitcnt lgkmcnt(3)\n"
      "v_mfma_f32_16x16x32_bf16 v[224:227], a[80:83], v[52:55], v[224:227]\n"
      "buffer_store_dwordx4 v[28:31], v5, s[40:43], s64 offen offset:64 sc1\n"
      "s_waitcnt lgkmcnt(2)\n"
      "v_mfma_f32_16x16x32_bf16 v[224:227], a[96:99], v[56:59], v[224:227]\n"
      "s_nop 3\n"
      "s_waitcnt lgkmcnt(1)\n"
      "v_mfma_f32_16x16x32_bf16 v[224:227], a[112:115], v[60:63], v[224:227]\n"
      "v_mfma_f32_16x16x32_bf16 v[228:231], a[120:123], v[60:63], v[228:231]\n"
      "ds_read_b128 v[36:39], v13 offset:14336\n"
      "v_mfma_f32_16x16x32_bf16 v[228:231], a[104:107], v[56:59], v[228:231]\n"
      "v_mfma_f32_16x16x32_bf16 v[228:231], a[88:91], v[52:55], v[228:231]\n"
      "s_nop 3\n"
      "v_mfma_f32_16x16x32_bf16 v[228:231], a[72:75], v[48:51], v[228:231]\n"
      "v_mfma_f32_16x16x32_bf16 v[220:223], a[76:79], v[48:51], v[220:223]\n"
      "ds_read_b128 v[40:43], v12 offset:47104\n"
      "v_mfma_f32_16x16x32_bf16 v[220:223], a[92:95], v[52:55], v[220:223]\n"
      "v_mfma_f32_16x16x32_bf16 v[220:223], a[108:111], v[56:59], v[220:223]\n"
      "s_nop 3\n"
      "v_mfma_f32_16x16x32_bf16 v[220:223], a[124:127], v[60:63], v[220:223]\n"
      "v_mfma_f32_16x16x32_bf16 v[216:219], a[116:119], v[60:63], v[216:219]\n"
      "ds_read_b128 v[44:47], v13 offset:47104\n"
      "v_mfma_f32_16x16x32_bf16 v[216:219], a[100:103], v[56:59], v[216:219]\n"
      "v_mfma_f32_16x16x32_bf16 v[216:219], a[84:87], v[52:55], v[216:219]\n"
      "s_nop 3\n"
      "v_mfma_f32_16x16x32_bf16 v[216:219], a[68:71], v[48:51], v[216:219]\n"
      "s_waitcnt lgkmcnt(3)\n"
      "v_mfma_f32_16x16x32_bf16 v[232:235], a[64:67], v[32:35], v[232:235]\n"
      "ds_read_b128 v[48:51], v12 offset:30720\n"
      "s_waitcnt lgkmcnt(3)\n"
      "v_mfma_f32_16x16x32_bf16 v[232:235], a[80:83], v[36:39], v[232:235]\n"
      "s_waitcnt lgkmcnt(2)\n"
      "v_mfma_f32_16x16x32_bf16 v[232:235], a[96:99], v[40:43], v[232:235]\n"
      "s_nop 3\n"
      "s_waitcnt lgkmcnt(1)\n"
      "v_mfma_f32_16x16x32_bf16 v[232:235], a[112:115], v[44:47], v[232:235]\n"
      "v_sub_f32 v208, v208, v216\n"
      "v_mfma_f32_16x16x32_bf16 v[236:239], a[120:123], v[44:47], v[236:239]\n"
      "ds_read_b128 v[52:55], v13 offset:30720\n"
      "v_sub_f32 v209, v209, v217\n"
      "v_mfma_f32_16x16x32_bf16 v[236:239], a[104:107], v[40:43], v[236:239]\n"
      "v_sub_f32 v210, v210, v218\n"
      "v_sub_f32 v211, v211, v219\n"
      "v_mfma_f32_16x16x32_bf16 v[236:239], a[88:91], v[36:39], v[236:239]\n"
      "v_cvt_pk_bf16_f32 v24, v208, v224\n"
      "v_cvt_pk_bf16_f32 v25, v209, v225\n"
      "v_mfma_f32_16x16x32_bf16 v[236:239], a[72:75], v[32:35], v[236:239]\n"
      "v_cvt_pk_bf16_f32 v26, v210, v226\n"
      "v_mfma_f32_16x16x32_bf16 v[252:255], a[76:79], v[32:35], v[252:255]\n"
      "ds_read_b128 v[56:59], v12 offset:63488\n"
      "v_cvt_pk_bf16_f32 v27, v211, v227\n"
      "v_mfma_f32_16x16x32_bf16 v[252:255], a[92:95], v[36:39], v[252:255]\n"
      "s_add_u32 s64, s61, 6094848\n"
      "buffer_store_dwordx4 v[24:27], v5, s[40:43], s64 offen offset:0 sc1\n"
      "v_mfma_f32_16x16x32_bf16 v[252:255], a[108:111], v[40:43], v[252:255]\n"
      "v_sub_f32 v212, v212, v220\n"
      "v_sub_f32 v213, v213, v221\n"
      "v_mfma_f32_16x16x32_bf16 v[252:255], a[124:127], v[44:47], v[252:255]\n"
      "v_sub_f32 v214, v214, v222\n"
      "v_mfma_f32_16x16x32_bf16 v[248:251], a[116:119], v[44:47], v[248:251]\n"
      "ds_read_b128 v[60:63], v13 offset:63488\n"
      "v_sub_f32 v215, v215, v223\n"
      "v_mfma_f32_16x16x32_bf16 v[248:251], a[100:103], v[40:43], v[248:251]\n"
      "v_cvt_pk_bf16_f32 v28, v212, v228\n"
      "v_cvt_pk_bf16_f32 v29, v213, v229\n"
      "v_mfma_f32_16x16x32_bf16 v[248:251], a[84:87], v[36:39], v[248:251]\n"
      "v_cvt_pk_bf16_f32 v30, v214, v230\n"
      "v_cvt_pk_bf16_f32 v31, v215, v231\n"
      "v_mfma_f32_16x16x32_bf16 v[248:251], a[68:71], v[32:35], v[248:251]\n"
      "s_waitcnt lgkmcnt(3)\n"
      "v_mfma_f32_16x16x32_bf16 v[248:251], a[64:67], v[48:51], v[248:251]\n"
      "s_waitcnt lgkmcnt(2)\n"
      "v_mfma_f32_16x16x32_bf16 v[248:251], a[80:83], v[52:55], v[248:251]\n"
      "buffer_store_dwordx4 v[28:31], v5, s[40:43], s64 offen offset:64 sc1\n"
      "s_waitcnt lgkmcnt(1)\n"
      "v_mfma_f32_16x16x32_bf16 v[248:251], a[96:99], v[56:59], v[248:251]\n"
      "s_nop 3\n"
      "s_waitcnt lgkmcnt(0)\n"
      "v_mfma_f32_16x16x32_bf16 v[248:251], a[112:115], v[60:63], v[248:251]\n"
      "v_mfma_f32_16x16x32_bf16 v[252:255], a[120:123], v[60:63], v[252:255]\n"
      "v_mfma_f32_16x16x32_bf16 v[252:255], a[104:107], v[56:59], v[252:255]\n"
      "v_mfma_f32_16x16x32_bf16 v[252:255], a[88:91], v[52:55], v[252:255]\n"
      "s_nop 3\n"
      "v_mfma_f32_16x16x32_bf16 v[252:255], a[72:75], v[48:51], v[252:255]\n"
      "v_mfma_f32_16x16x32_bf16 v[244:247], a[76:79], v[48:51], v[244:247]\n"
      "v_mfma_f32_16x16x32_bf16 v[244:247], a[92:95], v[52:55], v[244:247]\n"
      "v_mfma_f32_16x16x32_bf16 v[244:247], a[108:111], v[56:59], v[244:247]\n"
      "s_nop 3\n"
      "v_mfma_f32_16x16x32_bf16 v[244:247], a[124:127], v[60:63], v[244:247]\n"
      "v_mfma_f32_16x16x32_bf16 v[240:243], a[116:119], v[60:63], v[240:243]\n"
      "v_mfma_f32_16x16x32_bf16 v[240:243], a[100:103], v[56:59], v[240:243]\n"
      "v_mfma_f32_16x16x32_bf16 v[240:243], a[84:87], v[52:55], v[240:243]\n"
      "s_nop 3\n"
      "v_mfma_f32_16x16x32_bf16 v[240:243], a[68:71], v[48:51], v[240:243]\n"
      "s_nop 7\n"
      "v_sub_f32 v232, v232, v240\n"
      "v_sub_f32 v233, v233, v241\n"
      "v_sub_f32 v234, v234, v242\n"
      "v_sub_f32 v235, v235, v243\n"
      "v_cvt_pk_bf16_f32 v24, v232, v248\n"
      "v_cvt_pk_bf16_f32 v25, v233, v249\n"
      "v_cvt_pk_bf16_f32 v26, v234, v250\n"
      "v_cvt_pk_bf16_f32 v27, v235, v251\n"
      "s_add_u32 s64, s61, 7110656\n"
      "buffer_store_dwordx4 v[24:27], v6, s[40:43], s64 offen offset:0 sc1\n"
      "v_sub_f32 v236, v236, v244\n"
      "v_sub_f32 v237, v237, v245\n"
      "v_sub_f32 v238, v238, v246\n"
      "v_sub_f32 v239, v239, v247\n"
      "v_cvt_pk_bf16_f32 v28, v236, v252\n"
      "v_cvt_pk_bf16_f32 v29, v237, v253\n"
      "v_cvt_pk_bf16_f32 v30, v238, v254\n"
      "v_cvt_pk_bf16_f32 v31, v239, v255\n"
      "buffer_store_dwordx4 v[28:31], v6, s[40:43], s64 offen offset:64 sc1\n"
      "s_mov_b32 s73, 0x40000\n"
      "s_cmp_le_i32 s74, 0\n"
      "s_cselect_b32 s67, 0, 0x7fffff00\n"
      "v_or_b32 v23, s67, v18\n"
      "buffer_load_dword v19, v23, s[40:43], s69 offen sc1\n"
      "v_mov_b32 v224, 1.0\n"
      "v_mov_b32 v226, 0x3f317218\n"
      "v_mov_b32 v228, 0x3f800002\n"
      "v_mov_b32 v225, 1.0\n"
      "v_mov_b32 v227, 0x3f317218\n"
      "v_mov_b32 v229, 0x3f800002\n"
      "v_add_u32 v42, %[lds0], v2\n"
      "v_add_u32 v41, 0x24000, v42\n"
      "s_lshl_b32 s67, s52, 12\n"
      "v_add_u32 v40, s67, v42\n"
      "s_add_u32 s68, s67, 4096\n"
      "s_and_b32 s68, s68, 0x3fff\n"
      "v_add_u32 v45, s68, v42\n"
      "s_add_u32 s68, s67, 8192\n"
      "s_and_b32 s68, s68, 0x3fff\n"
      "v_add_u32 v46, s68, v42\n"
      "s_add_u32 s68, s67, 12288\n"
      "s_and_b32 s68, s68, 0x3fff\n"
      "v_add_u32 v47, s68, v42\n"
      "s_waitcnt vmcnt(5)\n"
      "s_barrier\n"
      "s_add_u32 s68, s66, 16384\n"
      "buffer_store_dword v20, v1, s[40:43], s68 offen sc1\n"
      "s_add_u32 s68, s66, 32768\n"
      "buffer_store_dword v20, v1, s[40:43], s68 offen sc1\n"
      "s_waitcnt vmcnt(3)\n"
      "s_barrier\n"
      "s_add_u32 s68, s66, 49152\n"
      "buffer_store_dword v20, v1, s[40:43], s68 offen sc1\n"
      "s_cmp_ge_u32 %[bid], 252\n"
      "s_cbranch_scc1 L_end_%=\n"
      "s_waitcnt vmcnt(3)\n"
      "s_branch L_late_%=\n"
      "L_idle_%=:\n"
      "v_lshrrev_b32 v23, 4, v2\n"
      "v_and_b32 v24, 15, v23\n"
      "v_lshrrev_b32 v25, 4, v23\n"
      "v_bfe_u32 v26, v23, 1, 3\n"
      "v_lshlrev_b32 v27, 7, v24\n"
      "v_add_u32 v27, %[lds0], v27\n"
      "v_or_b32 v28, 0, v25\n"
      "v_xor_b32 v29, v28, v26\n"
      "v_lshl_add_u32 v10, v29, 4, v27\n"
      "v_add_u32 v12, 0x10000, v10\n"
      "v_or_b32 v28, 4, v25\n"
      "v_xor_b32 v29, v28, v26\n"
      "v_lshl_add_u32 v11, v29, 4, v27\n"
      "v_add_u32 v13, 0x10000, v11\n"
      "s_lshl_b32 s67, s52, 12\n"
      "s_add_u32 s67, s67, 131072\n"
      "v_mul_u32_u24 v28, 0xf800, v24\n"
      "v_lshl_add_u32 v5, v25, 4, v28\n"
      "v_cmp_lt_u32 vcc, 13, v24\n"
      "v_mov_b32 v31, 0x7fffff00\n"
      "s_nop 0\n"
      "v_cndmask_b32 v6, v5, v31, vcc\n"
      "v_and_b32 v24, 31, v23\n"
      "v_lshrrev_b32 v25, 5, v23\n"
      "s_add_u32 s67, s67, %[lds0]\n"
      "v_bfe_u32 v26, v23, 1, 2\n"
      "v_lshl_add_u32 v27, v24, 6, s67\n"
      "v_or_b32 v28, 0, v25\n"
      "v_xor_b32 v29, v28, v26\n"
      "v_lshl_add_u32 v14, v29, 4, v27\n"
      "v_or_b32 v28, 2, v25\n"
      "v_xor_b32 v29, v28, v26\n"
      "v_lshl_add_u32 v15, v29, 4, v27\n"
      "v_lshrrev_b32 v28, 2, v23\n"
      "v_bfe_u32 v29, v23, 3, 2\n"
      "v_and_b32 v30, 3, v23\n"
      "v_xor_b32 v29, v29, v30\n"
      "v_lshlrev_b32 v29, 4, v29\n"
      "v_lshl_add_u32 v29, v28, 6, v29\n"
      "v_add_u32 v7, s67, v29\n"
      "v_lshlrev_b32 v30, 4, v30\n"
      "v_lshl_add_u32 v8, v28, 10, v30\n"
      "v_mov_b32 v20, 1\n"
      "v_lshlrev_b32 v29, 4, v24\n"
      "v_lshl_add_u32 v21, v25, 13, v29\n"
      "v_cmp_eq_u32 vcc, 1, v25\n"
      "s_lshl_b32 s67, s52, 14\n"
      "s_add_u32 s57, s57, s67\n"
      "v_cndmask_b32 v22, v21, v31, vcc\n"
      "s_add_u32 s59, %[lds0], s67\n"
      "s_sub_u32 s79, s57, s67\n"
      "s_lshr_b32 s67, s52, 1\n"
      "s_lshl_b32 s67, s67, 14\n"
      "s_and_b32 s68, s52, 1\n"
      "s_lshl_b32 s68, s68, 11\n"
      "s_add_u32 s67, s67, s68\n"
      "s_add_u32 s79, s79, s67\n"
      "s_add_u32 s97, %[lds0], s67\n"
      "s_add_u32 s78, s79, s68\n"
      "s_add_u32 s96, s97, s68\n"
      "s_mul_i32 s61, s54, 7999488\n"
      "s_lshl_b32 s67, s53, 11\n"
      "s_add_u32 s61, s61, s67\n"
      "s_lshl_b32 s67, s55, 7\n"
      "s_add_u32 s61, s61, s67\n"
      "s_add_u32 s61, s61, 0x1740000\n"
      "s_lshl_b32 s68, s54, 16\n"
      "s_lshr_b32 s67, s55, 2\n"
      "s_lshl_b32 s67, s67, 5\n"
      "s_add_u32 s67, s53, s67\n"
      "s_lshl_b32 s67, s67, 7\n"
      "s_add_u32 s66, s67, s68\n"
      "s_add_u32 s66, s66, 0x26a2000\n"
      "v_cmp_eq_u32 vcc, 0, %[tid]\n"
      "s_nop 1\n"
      "v_cndmask_b32 v1, v31, 0, vcc\n"
      "s_lshl_b32 s67, s52, 15\n"
      "s_add_u32 s65, s67, 0x2682000\n"
      "s_lshl_b32 s67, s52, 11\n"
      "s_add_u32 s67, s67, 147456\n"
      "s_add_u32 s75, s67, %[lds0]\n"
      "s_getpc_b64 s[50:51]\n"
      "s_add_u32 s48, s50, wn_tab@rel32@lo+32772\n"
      "s_addc_u32 s49, s51, wn_tab@rel32@hi+32780\n"
      "s_and_b32 s49, s49, 0xffff\n"
      "s_mov_b32 s50, 8192\n"
      "s_mov_b32 s51, 0x20000\n"
      "s_mov_b32 s44, s38\n"
      "s_and_b32 s45, s39, 0xffff\n"
      "s_mov_b32 s46, 4096000\n"
      "s_mov_b32 s47, 0x20000\n"
      "s_cmp_ge_u32 %[bid], 126\n"
      "s_cselect_b32 s67, 1, 0\n"
      "s_mul_i32 s68, s67, 126\n"
      "s_sub_u32 s68, %[bid], s68\n"
      "s_lshr_b32 s69, s68, 5\n"
      "s_mov_b32 s74, s69\n"
      "s_lshl2_add_u32 s69, s67, s69\n"
      "s_lshl_b32 s69, s69, 14\n"
      "s_add_u32 s69, s69, 0x26a2000\n"
      "s_lshl_b32 s72, s52, 12\n"
      "s_add_u32 s69, s69, s72\n"
      "v_lshlrev_b32 v18, 3, v2\n"
      "v_min_u32 v18, 0xf00, v18\n"
      "s_lshr_b32 s72, s68, 1\n"
      "s_and_b32 s68, s68, 1\n"
      "s_mul_i32 s67, s67, 4000\n"
      "s_add_u32 s67, s67, s68\n"
      "s_lshl_b32 s68, s72, 6\n"
      "s_add_u32 s67, s67, s68\n"
      "s_lshl_b32 s67, s67, 9\n"
      "s_lshl_b32 s68, s52, 7\n"
      "s_add_u32 s71, s67, s68\n"
      "s_mul_i32 s70, %[bid], 63488\n"
      "s_lshl_b32 s68, s52, 9\n"
      "s_add_u32 s70, s70, s68\n"
      "s_add_u32 s70, s70, 0x1740000\n"
      "s_cmp_ge_u32 %[bid], 252\n"
      "s_cbranch_scc1 L_end_%=\n"
      "s_mov_b32 m0, s75\n"
      "s_lshl_b32 s62, s52, 11\n"
      "buffer_load_dwordx4 v2, s[48:51], s62 offen offset:0 lds\n"
      "buffer_load_dwordx4 v2, s[48:51], s62 offen offset:1024 lds\n"
      "s_add_u32 s63, s65, 0\n"
      "buffer_load_dwordx4 a[128:131], v2, s[40:43], s63 offen offset:0\n"
      "buffer_load_dwordx4 a[132:135], v2, s[40:43], s63 offen offset:1024\n"
      "buffer_load_dwordx4 a[136:139], v2, s[40:43], s63 offen offset:2048\n"
      "buffer_load_dwordx4 a[140:143], v2, s[40:43], s63 offen offset:3072\n"
      "s_add_u32 s63, s65, 4096\n"
      "buffer_load_dwordx4 a[144:147], v2, s[40:43], s63 offen offset:0\n"
      "buffer_load_dwordx4 a[148:151], v2, s[40:43], s63 offen offset:1024\n"
      "buffer_load_dwordx4 a[152:155], v2, s[40:43], s63 offen offset:2048\n"
      "buffer_load_dwordx4 a[156:159], v2, s[40:43], s63 offen offset:3072\n"
      "s_add_u32 s63, s65, 8192\n"
      "buffer_load_dwordx4 a[160:163], v2, s[40:43], s63 offen offset:0\n"
      "buffer_load_dwordx4 a[164:167], v2, s[40:43], s63 offen offset:1024\n"
      "buffer_load_dwordx4 a[168:171], v2, s[40:43], s63 offen offset:2048\n"
      "buffer_load_dwordx4 a[172:175], v2, s[40:43], s63 offen offset:3072\n"
      "s_add_u32 s63, s65, 12288\n"
      "buffer_load_dwordx4 a[176:179], v2, s[40:43], s63 offen offset:0\n"
      "buffer_load_dwordx4 a[180:183], v2, s[40:43], s63 offen offset:1024\n"
      "buffer_load_dwordx4 a[184:187], v2, s[40:43], s63 offen offset:2048\n"
      "buffer_load_dwordx4 a[188:191], v2, s[40:43], s63 offen offset:3072\n"
      "s_add_u32 s63, s65, 16384\n"
      "buffer_load_dwordx4 a[192:195], v2, s[40:43], s63 offen offset:0\n"
      "buffer_load_dwordx4 a[196:199], v2, s[40:43], s63 offen offset:1024\n"
      "buffer_load_dwordx4 a[200:203], v2, s[40:43], s63 offen offset:2048\n"
      "buffer_load_dwordx4 a[204:207], v2, s[40:43], s63 offen offset:3072\n"
      "s_add_u32 s63, s65, 20480\n"
      "buffer_load_dwordx4 a[208:211], v2, s[40:43], s63 offen offset:0\n"
      "buffer_load_dwordx4 a[212:215], v2, s[40:43], s63 offen offset:1024\n"
      "buffer_load_dwordx4 a[216:219], v2, s[40:43], s63 offen offset:2048\n"
      "buffer_load_dwordx4 a[220:223], v2, s[40:43], s63 offen offset:3072\n"
      "s_add_u32 s63, s65, 24576\n"
      "buffer_load_dwordx4 a[224:227], v2, s[40:43], s63 offen offset:0\n"
      "buffer_load_dwordx4 a[228:231], v2, s[40:43], s63 offen offset:1024\n"
      "buffer_load_dwordx4 a[232:235], v2, s[40:43], s63 offen offset:2048\n"
      "buffer_load_dwordx4 a[236:239], v2, s[40:43], s63 offen offset:3072\n"
      "s_add_u32 s63, s65, 28672\n"
      "buffer_load_dwordx4 a[240:243], v2, s[40:43], s63 offen offset:0\n"
      "buffer_load_dwordx4 a[244:247], v2, s[40:43], s63 offen offset:1024\n"
      "buffer_load_dwordx4 a[248:251], v2, s[40:43], s63 offen offset:2048\n"
      "buffer_load_dwordx4 a[252:255], v2, s[40:43], s63 offen offset:3072\n"
      "s_mov_b32 s73, 0x40000\n"
      "v_mov_b32 v19, 0\n"
      "v_mov_b32 v224, 1.0\n"
      "v_mov_b32 v226, 0x3f317218\n"
      "v_mov_b32 v228, 0x3f800002\n"
      "v_mov_b32 v225, 1.0\n"
      "v_mov_b32 v227, 0x3f317218\n"
      "v_mov_b32 v229, 0x3f800002\n"
      "v_add_u32 v42, %[lds0], v2\n"
      "v_add_u32 v41, 0x24000, v42\n"
      "s_lshl_b32 s67, s52, 12\n"
      "v_add_u32 v40, s67, v42\n"
      "s_add_u32 s68, s67, 4096\n"
      "s_and_b32 s68, s68, 0x3fff\n"
      "v_add_u32 v45, s68, v42\n"
      "s_add_u32 s68, s67, 8192\n"
      "s_and_b32 s68, s68, 0x3fff\n"
      "v_add_u32 v46, s68, v42\n"
      "s_add_u32 s68, s67, 12288\n"
      "s_and_b32 s68, s68, 0x3fff\n"
      "v_add_u32 v47, s68, v42\n"
      "s_waitcnt vmcnt(0) lgkmcnt(0)\n"
      "s_barrier\n"
      "s_mov_b32 s74, 4\n"
      "s_sleep 127\n"
      "s_sleep 70\n"
      "s_branch L_pollB_%=\n"
      "L_late_%=:\n"
      "s_cmp_ge_u32 s74, 2\n"
      "s_cbranch_scc0 L_pollB_%=\n"
      "s_cmp_ge_u32 s74, 3\n"
      "s_cbranch_scc1 L_w1_%=\n"
      "s_sleep 10\n"
      "s_branch L_pollB_%=\n"
      "L_w1_%=:\n"
      "s_add_u32 s63, s65, 0\n"
      "buffer_load_dwordx4 a[128:131], v2, s[40:43], s63 offen offset:0\n"
      "buffer_load_dwordx4 a[132:135], v2, s[40:43], s63 offen offset:1024\n"
      "buffer_load_dwordx4 a[136:139], v2, s[40:43], s63 offen offset:2048\n"
      "buffer_load_dwordx4 a[140:143], v2, s[40:43], s63 offen offset:3072\n"
      "s_add_u32 s63, s65, 4096\n"
      "buffer_load_dwordx4 a[144:147], v2, s[40:43], s63 offen offset:0\n"
      "buffer_load_dwordx4 a[148:151], v2, s[40:43], s63 offen offset:1024\n"
      "buffer_load_dwordx4 a[152:155], v2, s[40:43], s63 offen offset:2048\n"
      "buffer_load_dwordx4 a[156:159], v2, s[40:43], s63 offen offset:3072\n"
      "s_add_u32 s63, s65, 8192\n"
      "buffer_load_dwordx4 a[160:163], v2, s[40:43], s63 offen offset:0\n"
      "buffer_load_dwordx4 a[164:167], v2, s[40:43], s63 offen offset:1024\n"
      "buffer_load_dwordx4 a[168:171], v2, s[40:43], s63 offen offset:2048\n"
      "buffer_load_dwordx4 a[172:175], v2, s[40:43], s63 offen offset:3072\n"
      "s_add_u32 s63, s65, 12288\n"
      "buffer_load_dwordx4 a[176:179], v2, s[40:43], s63 offen offset:0\n"
      "buffer_load_dwordx4 a[180:183], v2, s[40:43], s63 offen offset:1024\n"
      "buffer_load_dwordx4 a[184:187], v2, s[40:43], s63 offen offset:2048\n"
      "buffer_load_dwordx4 a[188:191], v2, s[40:43], s63 offen offset:3072\n"
      "L_pollB_%=:\n"
      "v_cmp_ne_u32 vcc, 1, v19\n"
      "s_sub_u32 s73, s73, 1\n"
      "s_cbranch_scc1 L_goB_%=\n"
      "s_cmp_eq_u64 vcc, 0\n"
      "s_cbranch_scc1 L_goB_%=\n"
      "buffer_load_dword v19, v18, s[40:43], s69 offen sc1\n"
      "s_sleep 12\n"
      "L_pl2B_%=:\n"
      "buffer_load_dword v23, v18, s[40:43], s69 offen sc1\n"
      "s_waitcnt vmcnt(1)\n"
      "v_cmp_ne_u32 vcc, 1, v19\n"
      "s_sub_u32 s73, s73, 1\n"
      "s_cbranch_scc1 L_goB_%=\n"
      "s_cmp_eq_u64 vcc, 0\n"
      "s_cbranch_scc1 L_goB_%=\n"
      "buffer_load_dword v19, v18, s[40:43], s69 offen sc1\n"
      "s_waitcnt vmcnt(1)\n"
      "v_cmp_ne_u32 vcc, 1, v23\n"
      "s_sub_u32 s73, s73, 1\n"
      "s_cbranch_scc1 L_goB_%=\n"
      "s_cmp_eq_u64 vcc, 0\n"
      "s_cbranch_scc1 L_goB_%=\n"
      "s_branch L_pl2B_%=\n"
      "L_goB_%=:\n"
      "s_add_u32 s62, s70, 0\n"
      "buffer_load_dwordx4 v[64:67], v21, s[40:43], s62 offen offset:0 sc1\n"
      "buffer_load_dwordx4 v[68:71], v21, s[40:43], s62 offen offset:2048 sc1\n"
      "s_add_u32 s62, s70, 4096\n"
      "buffer_load_dwordx4 v[72:75], v21, s[40:43], s62 offen offset:0 sc1\n"
      "buffer_load_dwordx4 v[76:79], v21, s[40:43], s62 offen offset:2048 sc1\n"
      "s_add_u32 s62, s70, 16384\n"
      "buffer_load_dwordx4 v[80:83], v21, s[40:43], s62 offen offset:0 sc1\n"
      "buffer_load_dwordx4 v[84:87], v21, s[40:43], s62 offen offset:2048 sc1\n"
      "s_add_u32 s62, s70, 20480\n"
      "buffer_load_dwordx4 v[88:91], v21, s[40:43], s62 offen offset:0 sc1\n"
      "buffer_load_dwordx4 v[92:95], v21, s[40:43], s62 offen offset:2048 sc1\n"
      "s_add_u32 s62, s70, 32768\n"
      "buffer_load_dwordx4 v[96:99], v21, s[40:43], s62 offen offset:0 sc1\n"
      "buffer_load_dwordx4 v[100:103], v21, s[40:43], s62 offen offset:2048 sc1\n"
      "s_add_u32 s62, s70, 36864\n"
      "buffer_load_dwordx4 v[104:107], v21, s[40:43], s62 offen offset:0 sc1\n"
      "buffer_load_dwordx4 v[108:111], v21, s[40:43], s62 offen offset:2048 sc1\n"
      "s_add_u32 s62, s70, 49152\n"
      "buffer_load_dwordx4 v[112:115], v21, s[40:43], s62 offen offset:0 sc1\n"
      "buffer_load_dwordx4 v[116:119], v21, s[40:43], s62 offen offset:2048 sc1\n"
      "s_add_u32 s62, s70, 53248\n"
      "buffer_load_dwordx4 v[120:123], v21, s[40:43], s62 offen offset:0 sc1\n"
      "buffer_load_dwordx4 v[124:127], v22, s[40:43], s62 offen offset:2048 sc1\n"
      "L_invA_%=:\n"
      "ds_read_b128 v[192:195], v41 offset:0\n"
      "ds_read_b128 v[196:199], v41 offset:1024\n"
      "ds_read_b128 v[200:203], v41 offset:2048\n"
      "ds_read_b128 v[204:207], v41 offset:3072\n"
      "ds_read_b128 v[208:211], v41 offset:4096\n"
      "ds_read_b128 v[212:215], v41 offset:5120\n"
      "ds_read_b128 v[216:219], v41 offset:6144\n"
      "ds_read_b128 v[220:223], v41 offset:7168\n"
      "s_waitcnt vmcnt(12)\n"
      "v_swap_b32 v65, v68\n"
      "v_swap_b32 v66, v72\n"
      "v_swap_b32 v67, v76\n"
      "v_swap_b32 v70, v73\n"
      "v_swap_b32 v71, v77\n"
      "v_swap_b32 v75, v78\n"
      "s_waitcnt lgkmcnt(6)\n"
      "s_nop 1\n"
      "v_mfma_f32_32x32x16_bf16 v[128:143], v[64:67], v[196:199], 0\n"
      "v_mfma_f32_32x32x16_bf16 v[128:143], v[64:67], v[192:195], v[128:143]\n"
      "s_cmp_lt_u32 s74, 3\n"
      "s_cbranch_scc0 L_ws0_%=\n"
      "s_add_u32 s63, s65, 0\n"
      "buffer_load_dwordx4 a[128:131], v2, s[40:43], s63 offen offset:0\n"
      "buffer_load_dwordx4 a[132:135], v2, s[40:43], s63 offen offset:1024\n"
      "buffer_load_dwordx4 a[136:139], v2, s[40:43], s63 offen offset:2048\n"
      "buffer_load_dwordx4 a[140:143], v2, s[40:43], s63 offen offset:3072\n"
      "L_ws0_%=:\n"
      "v_mfma_f32_32x32x16_bf16 v[144:159], v[68:71], v[196:199], 0\n"
      "v_mfma_f32_32x32x16_bf16 v[144:159], v[68:71], v[192:195], v[144:159]\n"
      "v_mfma_f32_32x32x16_bf16 v[160:175], v[72:75], v[196:199], 0\n"
      "v_mfma_f32_32x32x16_bf16 v[160:175], v[72:75], v[192:195], v[160:175]\n"
      "s_cbranch_scc1 L_yw1_%=\n"
      "s_waitcnt vmcnt(8)\n"
      "L_yw1_%=:\n"
      "s_waitcnt vmcnt(12)\n"
      "v_mfma_f32_32x32x16_bf16 v[176:191], v[76:79], v[196:199], 0\n"
      "v_swap_b32 v81, v84\n"
      "v_swap_b32 v82, v88\n"
      "v_swap_b32 v83, v92\n"
      "v_mfma_f32_32x32x16_bf16 v[176:191], v[76:79], v[192:195], v[176:191]\n"
      "v_swap_b32 v86, v89\n"
      "v_swap_b32 v87, v93\n"
      "v_swap_b32 v91, v94\n"
      "s_waitcnt lgkmcnt(4)\n"
      "s_nop 1\n"
      "v_mfma_f32_32x32x16_bf16 v[128:143], v[80:83], v[204:207], v[128:143]\n"
      "v_mfma_f32_32x32x16_bf16 v[128:143], v[80:83], v[200:203], v[128:143]\n"
      "s_cmp_lt_u32 s74, 3\n"
      "s_cbranch_scc0 L_ws1_%=\n"
      "s_add_u32 s63, s65, 4096\n"
      "buffer_load_dwordx4 a[144:147], v2, s[40:43], s63 offen offset:0\n"
      "buffer_load_dwordx4 a[148:151], v2, s[40:43], s63 offen offset:1024\n"
      "buffer_load_dwordx4 a[152:155], v2, s[40:43], s63 offen offset:2048\n"
      "buffer_load_dwordx4 a[156:159], v2, s[40:43], s63 offen offset:3072\n"
      "L_ws1_%=:\n"
      "v_mfma_f32_32x32x16_bf16 v[144:159], v[84:87], v[204:207], v[144:159]\n"
      "v_mfma_f32_32x32x16_bf16 v[144:159], v[84:87], v[200:203], v[144:159]\n"
      "v_mfma_f32_32x32x16_bf16 v[160:175], v[88:91], v[204:207], v[160:175]\n"
      "v_mfma_f32_32x32x16_bf16 v[160:175], v[88:91], v[200:203], v[160:175]\n"
      "s_cbranch_scc1 L_yw2_%=\n"
      "s_waitcnt vmcnt(4)\n"
      "L_yw2_%=:\n"
      "s_waitcnt vmcnt(12)\n"
      "v_mfma_f32_32x32x16_bf16 v[176:191], v[92:95], v[204:207], v[176:191]\n"
      "v_swap_b32 v97, v100\n"
      "v_swap_b32 v98, v104\n"
      "v_swap_b32 v99, v108\n"
      "v_mfma_f32_32x32x16_bf16 v[176:191], v[92:95], v[200:203], v[176:191]\n"
      "v_swap_b32 v102, v105\n"
      "v_swap_b32 v103, v109\n"
      "v_swap_b32 v107, v110\n"
      "s_waitcnt lgkmcnt(2)\n"
      "s_nop 1\n"
      "v_mfma_f32_32x32x16_bf16 v[128:143], v[96:99], v[212:215], v[128:143]\n"
      "v_mfma_f32_32x32x16_bf16 v[128:143], v[96:99], v[208:211], v[128:143]\n"
      "s_cmp_lt_u32 s74, 3\n"
      "s_cbranch_scc0 L_ws2_%=\n"
      "s_add_u32 s63, s65, 8192\n"
      "buffer_load_dwordx4 a[160:163], v2, s[40:43], s63 offen offset:0\n"
      "buffer_load_dwordx4 a[164:167], v2, s[40:43], s63 offen offset:1024\n"
      "buffer_load_dwordx4 a[168:171], v2, s[40:43], s63 offen offset:2048\n"
      "buffer_load_dwordx4 a[172:175], v2, s[40:43], s63 offen offset:3072\n"
      "L_ws2_%=:\n"
      "v_mfma_f32_32x32x16_bf16 v[144:159], v[100:103], v[212:215], v[144:159]\n"
      "v_mfma_f32_32x32x16_bf16 v[144:159], v[100:103], v[208:211], v[144:159]\n"
      "v_mfma_f32_32x32x16_bf16 v[160:175], v[104:107], v[212:215], v[160:175]\n"
      "v_mfma_f32_32x32x16_bf16 v[160:175], v[104:107], v[208:211], v[160:175]\n"
      "s_cbranch_scc1 L_yw3_%=\n"
      "s_waitcnt vmcnt(0)\n"
      "L_yw3_%=:\n"
      "s_waitcnt vmcnt(12)\n"
      "v_mfma_f32_32x32x16_bf16 v[176:191], v[108:111], v[212:215], v[176:191]\n"
      "v_swap_b32 v113, v116\n"
      "v_swap_b32 v114, v120\n"
      "v_swap_b32 v115, v124\n"
      "v_mfma_f32_32x32x16_bf16 v[176:191], v[108:111], v[208:211], v[176:191]\n"
      "v_swap_b32 v118, v121\n"
      "v_swap_b32 v119, v125\n"
      "v_swap_b32 v123, v126\n"
      "s_waitcnt lgkmcnt(0)\n"
      "s_nop 1\n"
      "v_mfma_f32_32x32x16_bf16 v[128:143], v[112:115], v[220:223], v[128:143]\n"
      "v_mfma_f32_32x32x16_bf16 v[128:143], v[112:115], v[216:219], v[128:143]\n"
      "v_mfma_f32_32x32x16_bf16 v[144:159], v[116:119], v[220:223], v[144:159]\n"
      "v_mfma_f32_32x32x16_bf16 v[144:159], v[116:119], v[216:219], v[144:159]\n"
      "s_cmp_lt_u32 s74, 3\n"
      "s_cbranch_scc0 L_ws3_%=\n"
      "s_add_u32 s63, s65, 12288\n"
      "buffer_load_dwordx4 a[176:179], v2, s[40:43], s63 offen offset:0\n"
      "buffer_load_dwordx4 a[180:183], v2, s[40:43], s63 offen offset:1024\n"
      "buffer_load_dwordx4 a[184:187], v2, s[40:43], s63 offen offset:2048\n"
      "buffer_load_dwordx4 a[188:191], v2, s[40:43], s63 offen offset:3072\n"
      "L_ws3_%=:\n"
      "s_nop 9\n"
      "v_exp_f32 v130, v130\n"
      "v_exp_f32 v131, v131\n"
      "v_exp_f32 v134, v134\n"
      "v_mfma_f32_32x32x16_bf16 v[160:175], v[120:123], v[220:223], v[160:175]\n"
      "v_exp_f32 v135, v135\n"
      "v_exp_f32 v138, v138\n"
      "v_exp_f32 v139, v139\n"
      "v_mfma_f32_32x32x16_bf16 v[160:175], v[120:123], v[216:219], v[160:175]\n"
      "v_exp_f32 v142, v142\n"
      "v_exp_f32 v143, v143\n"
      "v_exp_f32 v232, -|v128|\n"
      "v_mfma_f32_32x32x16_bf16 v[176:191], v[124:127], v[220:223], v[176:191]\n"
      "v_exp_f32 v233, -|v129|\n"
      "v_exp_f32 v234, -|v132|\n"
      "v_exp_f32 v235, -|v133|\n"
      "v_mfma_f32_32x32x16_bf16 v[176:191], v[124:127], v[216:219], v[176:191]\n"
      "v_exp_f32 v236, -|v136|\n"
      "v_exp_f32 v237, -|v137|\n"
      "v_exp_f32 v238, -|v140|\n"
      "v_exp_f32 v239, -|v141|\n"
      "v_pk_add_f32 v[130:131], v[130:131], v[224:225]\n"
      "v_pk_add_f32 v[134:135], v[134:135], v[224:225]\n"
      "v_pk_add_f32 v[138:139], v[138:139], v[224:225]\n"
      "v_pk_add_f32 v[142:143], v[142:143], v[224:225]\n"
      "v_pk_mul_f32 v[128:129], v[128:129], v[226:227]\n"
      "v_pk_mul_f32 v[132:133], v[132:133], v[226:227]\n"
      "v_pk_mul_f32 v[136:137], v[136:137], v[226:227]\n"
      "v_pk_mul_f32 v[140:141], v[140:141], v[226:227]\n"
      "v_pk_add_f32 v[240:241], v[232:233], v[224:225]\n"
      "v_pk_add_f32 v[242:243], v[234:235], v[224:225]\n"
      "v_pk_add_f32 v[244:245], v[236:237], v[224:225]\n"
      "v_pk_add_f32 v[246:247], v[238:239], v[224:225]\n"
      "v_pk_add_f32 v[232:233], v[228:229], v[232:233] neg_lo:[0,1] neg_hi:[0,1]\n"
      "v_pk_add_f32 v[234:235], v[228:229], v[234:235] neg_lo:[0,1] neg_hi:[0,1]\n"
      "v_pk_add_f32 v[236:237], v[228:229], v[236:237] neg_lo:[0,1] neg_hi:[0,1]\n"
      "v_pk_add_f32 v[238:239], v[228:229], v[238:239] neg_lo:[0,1] neg_hi:[0,1]\n"
      "v_pk_mul_f32 v[130:131], v[130:131], v[240:241]\n"
      "v_pk_mul_f32 v[134:135], v[134:135], v[242:243]\n"
      "v_pk_mul_f32 v[138:139], v[138:139], v[244:245]\n"
      "v_pk_mul_f32 v[142:143], v[142:143], v[246:247]\n"
      "v_rcp_f32 v130, v130\n"
      "v_rcp_f32 v131, v131\n"
      "v_rcp_f32 v134, v134\n"
      "v_rcp_f32 v135, v135\n"
      "v_rcp_f32 v138, v138\n"
      "v_rcp_f32 v139, v139\n"
      "v_rcp_f32 v142, v142\n"
      "v_rcp_f32 v143, v143\n"
      "v_med3_f32 v128, -v232, v128, v232\n"
      "v_med3_f32 v129, -v233, v129, v233\n"
      "v_med3_f32 v132, -v234, v132, v234\n"
      "v_med3_f32 v133, -v235, v133, v235\n"
      "v_med3_f32 v136, -v236, v136, v236\n"
      "v_med3_f32 v137, -v237, v137, v237\n"
      "v_med3_f32 v140, -v238, v140, v238\n"
      "v_med3_f32 v141, -v239, v141, v239\n"
      "v_pk_mul_f32 v[128:129], v[128:129], v[130:131]\n"
      "v_pk_mul_f32 v[132:133], v[132:133], v[134:135]\n"
      "v_pk_mul_f32 v[136:137], v[136:137], v[138:139]\n"
      "v_pk_mul_f32 v[140:141], v[140:141], v[142:143]\n"
      "s_cmp_lt_u32 s74, 4\n"
      "s_cbranch_scc0 L_ws4_%=\n"
      "s_add_u32 s63, s65, 16384\n"
      "buffer_load_dwordx4 a[192:195], v2, s[40:43], s63 offen offset:0\n"
      "buffer_load_dwordx4 a[196:199], v2, s[40:43], s63 offen offset:1024\n"
      "buffer_load_dwordx4 a[200:203], v2, s[40:43], s63 offen offset:2048\n"
      "buffer_load_dwordx4 a[204:207], v2, s[40:43], s63 offen offset:3072\n"
      "s_add_u32 s63, s65, 20480\n"
      "buffer_load_dwordx4 a[208:211], v2, s[40:43], s63 offen offset:0\n"
      "buffer_load_dwordx4 a[212:215], v2, s[40:43], s63 offen offset:1024\n"
      "L_ws4_%=:\n"
      "v_exp_f32 v146, v146\n"
      "v_exp_f32 v147, v147\n"
      "v_exp_f32 v150, v150\n"
      "v_exp_f32 v151, v151\n"
      "v_exp_f32 v154, v154\n"
      "v_exp_f32 v155, v155\n"
      "v_exp_f32 v158, v158\n"
      "v_exp_f32 v159, v159\n"
      "v_exp_f32 v232, -|v144|\n"
      "v_exp_f32 v233, -|v145|\n"
      "v_exp_f32 v234, -|v148|\n"
      "v_exp_f32 v235, -|v149|\n"
      "v_exp_f32 v236, -|v152|\n"
      "v_exp_f32 v237, -|v153|\n"
      "v_exp_f32 v238, -|v156|\n"
      "v_exp_f32 v239, -|v157|\n"
      "v_pk_add_f32 v[146:147], v[146:147], v[224:225]\n"
      "v_pk_add_f32 v[150:151], v[150:151], v[224:225]\n"
      "v_pk_add_f32 v[154:155], v[154:155], v[224:225]\n"
      "v_pk_add_f32 v[158:159], v[158:159], v[224:225]\n"
      "v_pk_mul_f32 v[144:145], v[144:145], v[226:227]\n"
      "v_pk_mul_f32 v[148:149], v[148:149], v[226:227]\n"
      "v_pk_mul_f32 v[152:153], v[152:153], v[226:227]\n"
      "v_pk_mul_f32 v[156:157], v[156:157], v[226:227]\n"
      "v_pk_add_f32 v[240:241], v[232:233], v[224:225]\n"
      "v_pk_add_f32 v[242:243], v[234:235], v[224:225]\n"
      "v_pk_add_f32 v[244:245], v[236:237], v[224:225]\n"
      "v_pk_add_f32 v[246:247], v[238:239], v[224:225]\n"
      "v_pk_add_f32 v[232:233], v[228:229], v[232:233] neg_lo:[0,1] neg_hi:[0,1]\n"
      "v_pk_add_f32 v[234:235], v[228:229], v[234:235] neg_lo:[0,1] neg_hi:[0,1]\n"
      "v_pk_add_f32 v[236:237], v[228:229], v[236:237] neg_lo:[0,1] neg_hi:[0,1]\n"
      "v_pk_add_f32 v[238:239], v[228:229], v[238:239] neg_lo:[0,1] neg_hi:[0,1]\n"
      "v_pk_mul_f32 v[146:147], v[146:147], v[240:241]\n"
      "v_pk_mul_f32 v[150:151], v[150:151], v[242:243]\n"
      "v_pk_mul_f32 v[154:155], v[154:155], v[244:245]\n"
      "v_pk_mul_f32 v[158:159], v[158:159], v[246:247]\n"
      "v_rcp_f32 v146, v146\n"
      "v_rcp_f32 v147, v147\n"
      "v_rcp_f32 v150, v150\n"
      "v_rcp_f32 v151, v151\n"
      "v_rcp_f32 v154, v154\n"
      "v_rcp_f32 v155, v155\n"
      "v_rcp_f32 v158, v158\n"
      "v_rcp_f32 v159, v159\n"
      "v_med3_f32 v144, -v232, v144, v232\n"
      "v_med3_f32 v145, -v233, v145, v233\n"
      "v_med3_f32 v148, -v234, v148, v234\n"
      "v_med3_f32 v149, -v235, v149, v235\n"
      "v_med3_f32 v152, -v236, v152, v236\n"
      "v_med3_f32 v153, -v237, v153, v237\n"
      "v_med3_f32 v156, -v238, v156, v238\n"
      "v_med3_f32 v157, -v239, v157, v239\n"
      "v_pk_mul_f32 v[144:145], v[144:145], v[146:147]\n"
      "v_pk_mul_f32 v[148:149], v[148:149], v[150:151]\n"
      "v_pk_mul_f32 v[152:153], v[152:153], v[154:155]\n"
      "v_pk_mul_f32 v[156:157], v[156:157], v[158:159]\n"
      "s_cmp_lt_u32 s74, 4\n"
      "s_cbranch_scc0 L_ws5_%=\n"
      "s_add_u32 s63, s65, 20480\n"
      "buffer_load_dwordx4 a[216:219], v2, s[40:43], s63 offen offset:2048\n"
      "buffer_load_dwordx4 a[220:223], v2, s[40:43], s63 offen offset:3072\n"
      "s_add_u32 s63, s65, 24576\n"
      "buffer_load_dwordx4 a[224:227], v2, s[40:43], s63 offen offset:0\n"
      "buffer_load_dwordx4 a[228:231], v2, s[40:43], s63 offen offset:1024\n"
      "buffer_load_dwordx4 a[232:235], v2, s[40:43], s63 offen offset:2048\n"
      "buffer_load_dwordx4 a[236:239], v2, s[40:43], s63 offen offset:3072\n"
      "L_ws5_%=:\n"
      "v_exp_f32 v162, v162\n"
      "v_exp_f32 v163, v163\n"
      "v_exp_f32 v166, v166\n"
      "v_exp_f32 v167, v167\n"
      "v_exp_f32 v170, v170\n"
      "v_exp_f32 v171, v171\n"
      "v_exp_f32 v174, v174\n"
      "v_exp_f32 v175, v175\n"
      "v_exp_f32 v232, -|v160|\n"
      "v_exp_f32 v233, -|v161|\n"
      "v_exp_f32 v234, -|v164|\n"
      "v_exp_f32 v235, -|v165|\n"
      "v_exp_f32 v236, -|v168|\n"
      "v_exp_f32 v237, -|v169|\n"
      "v_exp_f32 v238, -|v172|\n"
      "v_exp_f32 v239, -|v173|\n"
      "v_pk_add_f32 v[162:163], v[162:163], v[224:225]\n"
      "v_pk_add_f32 v[166:167], v[166:167], v[224:225]\n"
      "v_pk_add_f32 v[170:171], v[170:171], v[224:225]\n"
      "v_pk_add_f32 v[174:175], v[174:175], v[224:225]\n"
      "v_pk_mul_f32 v[160:161], v[160:161], v[226:227]\n"
      "v_pk_mul_f32 v[164:165], v[164:165], v[226:227]\n"
      "v_pk_mul_f32 v[168:169], v[168:169], v[226:227]\n"
      "v_pk_mul_f32 v[172:173], v[172:173], v[226:227]\n"
      "v_pk_add_f32 v[240:241], v[232:233], v[224:225]\n"
      "v_pk_add_f32 v[242:243], v[234:235], v[224:225]\n"
      "v_pk_add_f32 v[244:245], v[236:237], v[224:225]\n"
      "v_pk_add_f32 v[246:247], v[238:239], v[224:225]\n"
      "v_pk_add_f32 v[232:233], v[228:229], v[232:233] neg_lo:[0,1] neg_hi:[0,1]\n"
      "v_pk_add_f32 v[234:235], v[228:229], v[234:235] neg_lo:[0,1] neg_hi:[0,1]\n"
      "v_pk_add_f32 v[236:237], v[228:229], v[236:237] neg_lo:[0,1] neg_hi:[0,1]\n"
      "v_pk_add_f32 v[238:239], v[228:229], v[238:239] neg_lo:[0,1] neg_hi:[0,1]\n"
      "v_pk_mul_f32 v[162:163], v[162:163], v[240:241]\n"
      "v_pk_mul_f32 v[166:167], v[166:167], v[242:243]\n"
      "v_pk_mul_f32 v[170:171], v[170:171], v[244:245]\n"
      "v_pk_mul_f32 v[174:175], v[174:175], v[246:247]\n"
      "v_rcp_f32 v162, v162\n"
      "v_rcp_f32 v163, v163\n"
      "v_rcp_f32 v166, v166\n"
      "v_rcp_f32 v167, v167\n"
      "v_rcp_f32 v170, v170\n"
      "v_rcp_f32 v171, v171\n"
      "v_rcp_f32 v174, v174\n"
      "v_rcp_f32 v175, v175\n"
      "v_med3_f32 v160, -v232, v160, v232\n"
      "v_med3_f32 v161, -v233, v161, v233\n"
      "v_med3_f32 v164, -v234, v164, v234\n"
      "v_med3_f32 v165, -v235, v165, v235\n"
      "v_med3_f32 v168, -v236, v168, v236\n"
      "v_med3_f32 v169, -v237, v169, v237\n"
      "v_med3_f32 v172, -v238, v172, v238\n"
      "v_med3_f32 v173, -v239, v173, v239\n"
      "v_pk_mul_f32 v[160:161], v[160:161], v[162:163]\n"
      "v_pk_mul_f32 v[164:165], v[164:165], v[166:167]\n"
      "v_pk_mul_f32 v[168:169], v[168:169], v[170:171]\n"
      "v_pk_mul_f32 v[172:173], v[172:173], v[174:175]\n"
      "s_cmp_lt_u32 s74, 4\n"
      "s_cbranch_scc0 L_ws6_%=\n"
      "s_add_u32 s63, s65, 28672\n"
      "buffer_load_dwordx4 a[240:243], v2, s[40:43], s63 offen offset:0\n"
      "buffer_load_dwordx4 a[244:247], v2, s[40:43], s63 offen offset:1024\n"
      "buffer_load_dwordx4 a[248:251], v2, s[40:43], s63 offen offset:2048\n"
      "buffer_load_dwordx4 a[252:255], v2, s[40:43], s63 offen offset:3072\n"
      "L_ws6_%=:\n"
      "v_exp_f32 v178, v178\n"
      "v_exp_f32 v179, v179\n"
      "v_exp_f32 v182, v182\n"
      "v_exp_f32 v183, v183\n"
      "v_exp_f32 v186, v186\n"
      "v_exp_f32 v187, v187\n"
      "v_exp_f32 v190, v190\n"
      "v_exp_f32 v191, v191\n"
      "v_exp_f32 v232, -|v176|\n"
      "v_exp_f32 v233, -|v177|\n"
      "v_exp_f32 v234, -|v180|\n"
      "v_exp_f32 v235, -|v181|\n"
      "v_exp_f32 v236, -|v184|\n"
      "v_exp_f32 v237, -|v185|\n"
      "v_exp_f32 v238, -|v188|\n"
      "v_exp_f32 v239, -|v189|\n"
      "v_pk_add_f32 v[178:179], v[178:179], v[224:225]\n"
      "v_pk_add_f32 v[182:183], v[182:183], v[224:225]\n"
      "v_pk_add_f32 v[186:187], v[186:187], v[224:225]\n"
      "v_pk_add_f32 v[190:191], v[190:191], v[224:225]\n"
      "v_pk_mul_f32 v[176:177], v[176:177], v[226:227]\n"
      "v_pk_mul_f32 v[180:181], v[180:181], v[226:227]\n"
      "v_pk_mul_f32 v[184:185], v[184:185], v[226:227]\n"
      "v_pk_mul_f32 v[188:189], v[188:189], v[226:227]\n"
      "v_pk_add_f32 v[240:241], v[232:233], v[224:225]\n"
      "v_pk_add_f32 v[242:243], v[234:235], v[224:225]\n"
      "v_pk_add_f32 v[244:245], v[236:237], v[224:225]\n"
      "v_pk_add_f32 v[246:247], v[238:239], v[224:225]\n"
      "v_pk_add_f32 v[232:233], v[228:229], v[232:233] neg_lo:[0,1] neg_hi:[0,1]\n"
      "v_pk_add_f32 v[234:235], v[228:229], v[234:235] neg_lo:[0,1] neg_hi:[0,1]\n"
      "v_pk_add_f32 v[236:237], v[228:229], v[236:237] neg_lo:[0,1] neg_hi:[0,1]\n"
      "v_pk_add_f32 v[238:239], v[228:229], v[238:239] neg_lo:[0,1] neg_hi:[0,1]\n"
      "v_pk_mul_f32 v[178:179], v[178:179], v[240:241]\n"
      "v_pk_mul_f32 v[182:183], v[182:183], v[242:243]\n"
      "v_pk_mul_f32 v[186:187], v[186:187], v[244:245]\n"
      "v_pk_mul_f32 v[190:191], v[190:191], v[246:247]\n"
      "v_rcp_f32 v178, v178\n"
      "v_rcp_f32 v179, v179\n"
      "v_rcp_f32 v182, v182\n"
      "v_rcp_f32 v183, v183\n"
      "v_rcp_f32 v186, v186\n"
      "v_rcp_f32 v187, v187\n"
      "v_rcp_f32 v190, v190\n"
      "v_rcp_f32 v191, v191\n"
      "v_med3_f32 v176, -v232, v176, v232\n"
      "v_med3_f32 v177, -v233, v177, v233\n"
      "v_med3_f32 v180, -v234, v180, v234\n"
      "v_med3_f32 v181, -v235, v181, v235\n"
      "v_med3_f32 v184, -v236, v184, v236\n"
      "v_med3_f32 v185, -v237, v185, v237\n"
      "v_med3_f32 v188, -v238, v188, v238\n"
      "v_med3_f32 v189, -v239, v189, v239\n"
      "v_pk_mul_f32 v[176:177], v[176:177], v[178:179]\n"
      "v_pk_mul_f32 v[180:181], v[180:181], v[182:183]\n"
      "v_pk_mul_f32 v[184:185], v[184:185], v[186:187]\n"
      "v_pk_mul_f32 v[188:189], v[188:189], v[190:191]\n"
      "v_cvt_pk_bf16_f32 v24, v128, v144\n"
      "v_cvt_pk_bf16_f32 v25, v160, v176\n"
      "v_cvt_pk_bf16_f32 v26, v129, v145\n"
      "v_cvt_pk_bf16_f32 v27, v161, v177\n"
      "ds_write_b128 v40, v[24:27] offset:0\n"
      "v_cvt_pk_bf16_f32 v28, v132, v148\n"
      "v_cvt_pk_bf16_f32 v29, v164, v180\n"
      "v_cvt_pk_bf16_f32 v30, v133, v149\n"
      "v_cvt_pk_bf16_f32 v31, v165, v181\n"
      "ds_write_b128 v40, v[28:31] offset:1024\n"
      "v_cvt_pk_bf16_f32 v32, v136, v152\n"
      "v_cvt_pk_bf16_f32 v33, v168, v184\n"
      "v_cvt_pk_bf16_f32 v34, v137, v153\n"
      "v_cvt_pk_bf16_f32 v35, v169, v185\n"
      "ds_write_b128 v40, v[32:35] offset:2048\n"
      "v_cvt_pk_bf16_f32 v36, v140, v156\n"
      "v_cvt_pk_bf16_f32 v37, v172, v188\n"
      "v_cvt_pk_bf16_f32 v38, v141, v157\n"
      "v_cvt_pk_bf16_f32 v39, v173, v189\n"
      "ds_write_b128 v40, v[36:39] offset:3072\n"
      "s_waitcnt vmcnt(28)\n"
      "v_mfma_f32_32x32x16_bf16 v[192:207], a[128:131], v[24:27], 0\n"
      "v_mfma_f32_32x32x16_bf16 v[192:207], a[132:135], v[28:31], v[192:207]\n"
      "v_mfma_f32_32x32x16_bf16 v[192:207], a[136:139], v[32:35], v[192:207]\n"
      "s_waitcnt lgkmcnt(0)\n"
      "s_barrier\n"
      "v_mfma_f32_32x32x16_bf16 v[192:207], a[140:143], v[36:39], v[192:207]\n"
      "ds_read_b128 v[80:83], v45 offset:0\n"
      "ds_read_b128 v[84:87], v45 offset:1024\n"
      "ds_read_b128 v[88:91], v45 offset:2048\n"
      "ds_read_b128 v[92:95], v45 offset:3072\n"
      "ds_read_b128 v[96:99], v46 offset:0\n"
      "ds_read_b128 v[100:103], v46 offset:1024\n"
      "ds_read_b128 v[104:107], v46 offset:2048\n"
      "ds_read_b128 v[108:111], v46 offset:3072\n"
      "ds_read_b128 v[112:115], v47 offset:0\n"
      "ds_read_b128 v[116:119], v47 offset:1024\n"
      "ds_read_b128 v[120:123], v47 offset:2048\n"
      "ds_read_b128 v[124:127], v47 offset:3072\n"
      "s_waitcnt vmcnt(12)\n"
      "v_mfma_f32_32x32x16_bf16 v[208:223], a[192:195], v[24:27], 0\n"
      "v_mfma_f32_32x32x16_bf16 v[208:223], a[196:199], v[28:31], v[208:223]\n"
      "v_mfma_f32_32x32x16_bf16 v[208:223], a[200:203], v[32:35], v[208:223]\n"
      "v_mfma_f32_32x32x16_bf16 v[208:223], a[204:207], v[36:39], v[208:223]\n"
      "s_add_u32 s64, s71, 16384\n"
      "s_cmp_eq_u32 s72, 62\n"
      "s_cselect_b32 s67, 0x7fffff00, 0\n"
      "v_or_b32 v9, s67, v8\n"
      "s_waitcnt lgkmcnt(11)\n"
      "v_mfma_f32_32x32x16_bf16 v[192:207], a[144:147], v[80:83], v[192:207]\n"
      "s_waitcnt lgkmcnt(10)\n"
      "v_mfma_f32_32x32x16_bf16 v[192:207], a[148:151], v[84:87], v[192:207]\n"
      "s_waitcnt lgkmcnt(9)\n"
      "v_mfma_f32_32x32x16_bf16 v[192:207], a[152:155], v[88:91], v[192:207]\n"
      "s_waitcnt lgkmcnt(8)\n"
      "v_mfma_f32_32x32x16_bf16 v[192:207], a[156:159], v[92:95], v[192:207]\n"
      "s_waitcnt lgkmcnt(7)\n"
      "v_mfma_f32_32x32x16_bf16 v[192:207], a[160:163], v[96:99], v[192:207]\n"
      "s_waitcnt lgkmcnt(6)\n"
      "v_mfma_f32_32x32x16_bf16 v[192:207], a[164:167], v[100:103], v[192:207]\n"
      "s_waitcnt lgkmcnt(5)\n"
      "v_mfma_f32_32x32x16_bf16 v[192:207], a[168:171], v[104:107], v[192:207]\n"
      "s_waitcnt lgkmcnt(4)\n"
      "v_mfma_f32_32x32x16_bf16 v[192:207], a[172:175], v[108:111], v[192:207]\n"
      "s_waitcnt lgkmcnt(3)\n"
      "v_mfma_f32_32x32x16_bf16 v[192:207], a[176:179], v[112:115], v[192:207]\n"
      "s_waitcnt lgkmcnt(2)\n"
      "v_mfma_f32_32x32x16_bf16 v[192:207], a[180:183], v[116:119], v[192:207]\n"
      "s_waitcnt lgkmcnt(1)\n"
      "v_mfma_f32_32x32x16_bf16 v[192:207], a[184:187], v[120:123], v[192:207]\n"
      "s_waitcnt lgkmcnt(0)\n"
      "v_mfma_f32_32x32x16_bf16 v[192:207], a[188:191], v[124:127], v[192:207]\n"
      "s_waitcnt vmcnt(10)\n"
      "v_mfma_f32_32x32x16_bf16 v[208:223], a[208:211], v[80:83], v[208:223]\n"
      "v_mfma_f32_32x32x16_bf16 v[208:223], a[212:215], v[84:87], v[208:223]\n"
      "s_nop 3\n"
      "s_waitcnt vmcnt(8)\n"
      "v_mfma_f32_32x32x16_bf16 v[208:223], a[216:219], v[88:91], v[208:223]\n"
      "v_mfma_f32_32x32x16_bf16 v[208:223], a[220:223], v[92:95], v[208:223]\n"
      "s_waitcnt vmcnt(6)\n"
      "v_mfma_f32_32x32x16_bf16 v[208:223], a[224:227], v[96:99], v[208:223]\n"
      "v_cvt_pk_bf16_f32 v48, v192, v193\n"
      "v_cvt_pk_bf16_f32 v49, v194, v195\n"
      "v_mfma_f32_32x32x16_bf16 v[208:223], a[228:231], v[100:103], v[208:223]\n"
      "v_cvt_pk_bf16_f32 v50, v196, v197\n"
      "v_cvt_pk_bf16_f32 v51, v198, v199\n"
      "s_waitcnt vmcnt(4)\n"
      "v_mfma_f32_32x32x16_bf16 v[208:223], a[232:235], v[104:107], v[208:223]\n"
      "ds_write_b128 v14, v[48:51] offset:0\n"
      "v_cvt_pk_bf16_f32 v52, v200, v201\n"
      "v_mfma_f32_32x32x16_bf16 v[208:223], a[236:239], v[108:111], v[208:223]\n"
      "v_cvt_pk_bf16_f32 v53, v202, v203\n"
      "v_cvt_pk_bf16_f32 v54, v204, v205\n"
      "s_waitcnt vmcnt(2)\n"
      "v_mfma_f32_32x32x16_bf16 v[208:223], a[240:243], v[112:115], v[208:223]\n"
      "v_cvt_pk_bf16_f32 v55, v206, v207\n"
      "ds_write_b128 v15, v[52:55] offset:0\n"
      "v_mfma_f32_32x32x16_bf16 v[208:223], a[244:247], v[116:119], v[208:223]\n"
      "ds_read_b128 v[48:51], v7 offset:0\n"
      "ds_read_b128 v[52:55], v7 offset:1024\n"
      "s_waitcnt vmcnt(0)\n"
      "v_mfma_f32_32x32x16_bf16 v[208:223], a[248:251], v[120:123], v[208:223]\n"
      "v_mfma_f32_32x32x16_bf16 v[208:223], a[252:255], v[124:127], v[208:223]\n"
      "s_waitcnt lgkmcnt(1)\n"
      "buffer_store_dwordx4 v[48:51], v8, s[44:47], s71 offen offset:0 sc1\n"
      "s_waitcnt lgkmcnt(0)\n"
      "buffer_store_dwordx4 v[52:55], v9, s[44:47], s64 offen offset:0 sc1\n"
      "s_nop 7\n"
      "v_cvt_pk_bf16_f32 v56, v208, v209\n"
      "v_cvt_pk_bf16_f32 v57, v210, v211\n"
      "v_cvt_pk_bf16_f32 v58, v212, v213\n"
      "v_cvt_pk_bf16_f32 v59, v214, v215\n"
      "ds_write_b128 v14, v[56:59] offset:2048\n"
      "v_cvt_pk_bf16_f32 v60, v216, v217\n"
      "v_cvt_pk_bf16_f32 v61, v218, v219\n"
      "v_cvt_pk_bf16_f32 v62, v220, v221\n"
      "v_cvt_pk_bf16_f32 v63, v222, v223\n"
      "ds_write_b128 v15, v[60:63] offset:2048\n"
      "ds_read_b128 v[56:59], v7 offset:2048\n"
      "ds_read_b128 v[60:63], v7 offset:3072\n"
      "s_waitcnt lgkmcnt(1)\n"
      "buffer_store_dwordx4 v[56:59], v8, s[44:47], s71 offen offset:64 sc1\n"
      "s_waitcnt lgkmcnt(0)\n"
      "buffer_store_dwordx4 v[60:63], v9, s[44:47], s64 offen offset:64 sc1\n"
      "L_end_%=:\n"
      "wn_code_hi:\n"
      :
      : [ka] "s"(__builtin_amdgcn_kernarg_segment_ptr()), [bid] "s"(blockIdx.x), [tid] "v"(threadIdx.x),
        [lds0] "s"((unsigned)(size_t)(LDSP char*)smem)
      : "memory", "scc", "v1", "v2", "v3", "v4", "v5", "v6", "v7", "v8", "v9", "v10", "v11", "v12", "v13", "v14", "v15", "v16",
        "v17", "v18", "v19", "v20", "v21", "v22", "v23", "v24", "v25", "v26", "v27", "v28", "v29", "v30", "v31", "v32",
        "v33", "v34", "v35", "v36", "v37", "v38", "v39", "v40", "v41", "v42", "v43", "v44", "v45", "v46", "v47", "v48",
        "v49", "v50", "v51", "v52", "v53", "v54", "v55", "v56", "v57", "v58", "v59", "v60", "v61", "v62", "v63", "v64",
        "v65", "v66", "v67", "v68", "v69", "v70", "v71", "v72", "v73", "v74", "v75", "v76", "v77", "v78", "v79", "v80",
        "v81", "v82", "v83", "v84", "v85", "v86", "v87", "v88", "v89", "v90", "v91", "v92", "v93", "v94", "v95", "v96",
        "v97", "v98", "v99", "v100", "v101", "v102", "v103", "v104", "v105", "v106", "v107", "v108", "v109", "v110", "v111", "v112",
        "v113", "v114", "v115", "v116", "v117", "v118", "v119", "v120", "v121", "v122", "v123", "v124", "v125", "v126", "v127", "v128",
        "v129", "v130", "v131", "v132", "v133", "v134", "v135", "v136", "v137", "v138", "v139", "v140", "v141", "v142", "v143", "v144",
        "v145", "v146", "v147", "v148", "v149", "v150", "v151", "v152", "v153", "v154", "v155", "v156", "v157", "v158", "v159", "v160",
        "v161", "v162", "v163", "v164", "v165", "v166", "v167", "v168", "v169", "v170", "v171", "v172", "v173", "v174", "v175", "v176",
        "v177", "v178", "v179", "v180", "v181", "v182", "v183", "v184", "v185", "v186", "v187", "v188", "v189", "v190", "v191", "v192",
        "v193", "v194", "v195", "v196", "v197", "v198", "v199", "v200", "v201", "v202", "v203", "v204", "v205", "v206", "v207", "v208",
        "v209", "v210", "v211", "v212", "v213", "v214", "v215", "v216", "v217", "v218", "v219", "v220", "v221", "v222", "v223", "v224",
        "v225", "v226", "v227", "v228", "v229", "v230", "v231", "v232", "v233", "v234", "v235", "v236", "v237", "v238", "v239", "v240",
        "v241", "v242", "v243", "v244", "v245", "v246", "v247", "v248", "v249", "v250", "v251", "v252", "v253", "v254", "v255", "a0",
        "a1", "a2", "a3", "a4", "a5", "a6", "a7", "a8", "a9", "a10", "a11", "a12", "a13", "a14", "a15", "a16",
        "a17", "a18", "a19", "a20", "a21", "a22", "a23", "a24", "a25", "a26", "a27", "a28", "a29", "a30", "a31", "a32",
        "a33", "a34", "a35", "a36", "a37", "a38", "a39", "a40", "a41", "a42", "a43", "a44", "a45", "a46", "a47", "a48",
        "a49", "a50", "a51", "a52", "a53", "a54", "a55", "a56", "a57", "a58", "a59", "a60", "a61", "a62", "a63", "a64",
        "a65", "a66", "a67", "a68", "a69", "a70", "a71", "a72", "a73", "a74", "a75", "a76", "a77", "a78", "a79", "a80",
        "a81", "a82", "a83", "a84", "a85", "a86", "a87", "a88", "a89", "a90", "a91", "a92", "a93", "a94", "a95", "a96",
        "a97", "a98", "a99", "a100", "a101", "a102", "a103", "a104", "a105", "a106", "a107", "a108", "a109", "a110", "a111", "a112",
        "a113", "a114", "a115", "a116", "a117", "a118", "a119", "a120", "a121", "a122", "a123", "a124", "a125", "a126", "a127", "a128",
        "a129", "a130", "a131", "a132", "a133", "a134", "a135", "a136", "a137", "a138", "a139", "a140", "a141", "a142", "a143", "a144",
        "a145", "a146", "a147", "a148", "a149", "a150", "a151", "a152", "a153", "a154", "a155", "a156", "a157", "a158", "a159", "a160",
        "a161", "a162", "a163", "a164", "a165", "a166", "a167", "a168", "a169", "a170", "a171", "a172", "a173", "a174", "a175", "a176",
        "a177", "a178", "a179", "a180", "a181", "a182", "a183", "a184", "a185", "a186", "a187", "a188", "a189", "a190", "a191", "a192",
        "a193", "a194", "a195", "a196", "a197", "a198", "a199", "a200", "a201", "a202", "a203", "a204", "a205", "a206", "a207", "a208",
        "a209", "a210", "a211", "a212", "a213", "a214", "a215", "a216", "a217", "a218", "a219", "a220", "a221", "a222", "a223", "a224",
        "a225", "a226", "a227", "a228", "a229", "a230", "a231", "a232", "a233", "a234", "a235", "a236", "a237", "a238", "a239", "a240",
        "a241", "a242", "a243", "a244", "a245", "a246", "a247", "a248", "a249", "a250", "a251", "a252", "a253", "a254", "a255", "s36",
        "s37", "s38", "s39", "s40", "s41", "s42", "s43", "s44", "s45", "s46", "s47", "s48", "s49", "s50", "s51", "s52",
        "s53", "s54", "s55", "s56", "s57", "s58", "s59", "s60", "s61", "s62", "s63", "s64", "s65", "s66", "s67", "s68",
        "s69", "s70", "s71", "s72", "s73", "s74", "s75", "s76", "s77", "s78", "s79", "s80", "s81", "s82", "s83", "s84",
        "s85", "s86", "s87", "s88", "s89", "s90", "s91", "s92", "s93", "s94", "s95", "s96", "s97", "s98", "s99", "vcc");
}

extern "C" void kernel_launch(void* const* d_in, const int* in_sizes, int n_in,
                              void* d_out, int out_size, void* d_ws, size_t ws_size,
                              hipStream_t stream) {
  k1_fwd<<<256, 512, 0, stream>>>((const u16*)d_in[0], (const u16*)d_in[1], (const u16*)d_in[2], (char*)d_ws);
  k23<<<256, 256, 0, stream>>>((char*)d_ws, (u16*)d_out);
}
```
